```python
import torch

def _softmax(x, axis=-1):
    return torch.softmax(x, dim=axis)
def _gelu(x):
    return torch.nn.functional.gelu(x, approximate="tanh")
def _silu(x):
    return torch.nn.functional.silu(x)
def _sigmoid(x):
    return torch.sigmoid(x)
def _layernorm(x, axis=-1, eps=1e-5):
    m = x.mean(dim=axis, keepdim=True)
    v = x.var(dim=axis, keepdim=True, unbiased=False)
    return (x - m) / torch.sqrt(v + eps)
def _scatter_add(tgt, idx, src, axis=0):
    return tgt.clone().index_add_(axis, idx, src)
def _segment_sum(src, seg, n):
    out = torch.zeros((n,) + src.shape[1:], dtype=src.dtype)
    return out.index_add_(0, seg, src)
def _pad(x, pw):
    flat = tuple(v for pair in reversed(pw) for v in pair)
    return torch.nn.functional.pad(x, flat)

kernel_name = 'parallel_sum__ssm_conv_s4__mixer_channel_mlp__prompt'

def setup_inputs():

    _w = (lambda a: (lambda t: t.to(torch.bfloat16) if t.is_floating_point() else t)(torch.as_tensor(a)))
    def _A_setup():
        B, L, D, N = 1, 2048, 448, 256
        A_re = -(lambda _x: torch.exp(torch.as_tensor(_x)))((lambda *s: torch.randn(*s, dtype=torch.bfloat16) * 3.2)(D, N))
        A_im = torch.pi * (lambda *s: torch.randn(*s, dtype=torch.bfloat16) * 0.05)(D, N)
        B_re = (lambda *s: torch.randn(*s, dtype=torch.bfloat16) * 0.1)(D, N)
        B_im = (lambda *s: torch.randn(*s, dtype=torch.bfloat16) * 0.1)(D, N)
        C_re = (lambda *s: torch.randn(*s, dtype=torch.bfloat16) * 0.1)(D, N)
        C_im = (lambda *s: torch.randn(*s, dtype=torch.bfloat16) * 0.1)(D, N)
        log_dt = (lambda *s: torch.randn(*s, dtype=torch.bfloat16) * 0.05)(D) * 0.5 - 4.0
        Dskip = (lambda *s: torch.randn(*s, dtype=torch.bfloat16) * 0.05)(D)
        return {
            "u": _w((lambda *s: torch.randn(*s, dtype=torch.bfloat16) * 0.05)(B, L, D)),
            "A_re": _w(A_re),
            "A_im": _w(A_im),
            "B_re": _w(B_re),
            "B_im": _w(B_im),
            "C_re": _w(C_re),
            "C_im": _w(C_im),
            "log_dt": _w(log_dt),
            "Dskip": _w(Dskip),
        }
    _A_in = _A_setup()

    def _B_setup():
        B, S, D, F = 1, 2048, 448, 1792
        return {
            "x": _w((lambda *s: torch.randn(*s, dtype=torch.bfloat16) * 0.05)(B, S, D)),
            "w1": _w((lambda *s: torch.randn(*s, dtype=torch.bfloat16) * 3.2)(D, F) * (D ** -0.5)),
            "b1": _w((lambda *s: torch.randn(*s, dtype=torch.bfloat16) * 0.8)(F)),
            "w2": _w((lambda *s: torch.randn(*s, dtype=torch.bfloat16) * 0.05)(F, D) * (F ** -0.5)),
            "b2": _w((lambda *s: torch.randn(*s, dtype=torch.bfloat16) * 0.05)(D)),
        }
    _B_in = _B_setup()

    return {
        "A_u": _A_in["u"],
        "A_A_re": _A_in["A_re"],
        "A_A_im": _A_in["A_im"],
        "A_B_re": _A_in["B_re"],
        "A_B_im": _A_in["B_im"],
        "A_C_re": _A_in["C_re"],
        "A_C_im": _A_in["C_im"],
        "A_log_dt": _A_in["log_dt"],
        "A_Dskip": _A_in["Dskip"],
        "B_x": _B_in["x"],
        "B_w1": _B_in["w1"],
        "B_b1": _B_in["b1"],
        "B_w2": _B_in["w2"],
        "B_b2": _B_in["b2"],
    }

def reference(A_u, A_A_re, A_A_im, A_B_re, A_B_im, A_C_re, A_C_im, A_log_dt, A_Dskip, B_x, B_w1, B_b1, B_w2, B_b2):
    def _A_ref(u, A_re, A_im, B_re, B_im, C_re, C_im, log_dt, Dskip):
        Bsz, L, D = u.shape
        dt = torch.exp(log_dt)[:, None]
        A = A_re + 1j * A_im
        Bc = B_re + 1j * B_im
        Cc = C_re + 1j * C_im
        logdA = dt * A
        dB = dt * Bc
        l = (lambda x, dt: x.to(dt))(torch.arange(L), A_re.dtype)
        P = torch.exp(logdA[:, :, None] * l[None, None, :])
        K = torch.einsum("dn,dnl,dn->dl", Cc, P, dB).real
        L2 = 2 * L
        Kp = _pad(K, ((0, 0), (0, L)))
        up = (lambda x, *d: x.permute(*d))(u, 0, 2, 1)
        up = _pad(up, ((0, 0), (0, 0), (0, L)))
        Kf = (lambda x, axes=(-2,-1): torch.fft.rfft2(x.float(), dim=axes))(Kp[:, None, :], axes=(-2, -1))
        Uf = (lambda x, axes=(-2,-1): torch.fft.rfft2(x.float(), dim=axes))(up[:, :, None, :], axes=(-2, -1))
        Yf = Uf * Kf[None, :, :, :]
        y = (lambda x, s, axes=(-2,-1): torch.fft.irfft2(x, s=s, dim=axes).to(torch.bfloat16))(Yf, (1, L2), axes=(-2, -1))[:, :, 0, :L]
        y = (lambda x, *d: x.permute(*d))(y, 0, 2, 1) + u * Dskip[None, None, :]
        return y

    def _B_ref(x, w1, b1, w2, b2):
        y = _layernorm(x, axis=-1)
        h = _gelu(torch.einsum("bsd,df->bsf", y, w1) + b1)
        return torch.einsum("bsf,fd->bsd", h, w2) + b2

    x = A_u
    xn = _layernorm(x, axis=-1)
    return x + _A_ref(xn, A_A_re, A_A_im, A_B_re, A_B_im, A_C_re, A_C_im, A_log_dt, A_Dskip) + _B_ref(xn, B_w1, B_b1, B_w2, B_b2)
```

```cpp
#include <hip/hip_runtime.h>
#include <hip/hip_bf16.h>
#include <stdint.h>

typedef unsigned short u16;
__device__ __forceinline__ float bf2f(u16 v) { return __uint_as_float(((unsigned)v) << 16); }
__device__ __forceinline__ u16 f2bf(float f) { __hip_bfloat16 b = __float2bfloat16(f); return *reinterpret_cast<u16*>(&b); }
__device__ __forceinline__ float rbf(float f) { return bf2f(f2bf(f)); }

constexpr int L = 2048, D = 448, N = 256, F = 1792;

__device__ float wave_sum(float v) { for (int o = 32; o > 0; o >>= 1) v += __shfl_xor(v, o); return v; }

__global__ void k_ln(const u16* x, u16* xn, u16* y2) {
  int row = blockIdx.x, lane = threadIdx.x;
  float v[7];
  for (int i = 0; i < 7; ++i) v[i] = bf2f(x[row * D + lane + 64 * i]);
  for (int pass = 0; pass < 2; ++pass) {
    float s = 0; for (int i = 0; i < 7; ++i) s += v[i];
    float mean = wave_sum(s) / D;
    float q = 0; for (int i = 0; i < 7; ++i) q += (v[i] - mean) * (v[i] - mean);
    float var = wave_sum(q) / D;
    float m = rbf(mean), vv = rbf(var);
    float sd = rbf(sqrtf(rbf(vv + 1e-5f)));
    for (int i = 0; i < 7; ++i) v[i] = rbf(rbf(v[i] - m) / sd);
    u16* o = pass ? y2 : xn;
    for (int i = 0; i < 7; ++i) o[row * D + lane + 64 * i] = f2bf(v[i]);
  }
}

__global__ void k_kgen(const u16* Are, const u16* Aim, const u16* Bre, const u16* Bim, const u16* Cre, const u16* Cim, const u16* logdt, float* K) {
  int d = blockIdx.x;
  __shared__ float ar[N], ai[N], cr[N], ci[N];
  float dt = rbf(expf(bf2f(logdt[d])));
  for (int n = threadIdx.x; n < N; n += blockDim.x) {
    ar[n] = dt * bf2f(Are[d * N + n]); ai[n] = dt * bf2f(Aim[d * N + n]);
    float br = dt * bf2f(Bre[d * N + n]), bi = dt * bf2f(Bim[d * N + n]);
    float c_r = bf2f(Cre[d * N + n]), c_i = bf2f(Cim[d * N + n]);
    cr[n] = c_r * br - c_i * bi; ci[n] = c_r * bi + c_i * br;
  }
  __syncthreads();
  for (int l = threadIdx.x; l < L; l += blockDim.x) {
    float lr = rbf((float)l);
    float acc = 0;
    for (int n = 0; n < N; ++n) {
      float e = expf(ar[n] * lr), s, c; sincosf(ai[n] * lr, &s, &c);
      acc += cr[n] * (e * c) - ci[n] * (e * s);
    }
    K[d * L + l] = acc;
  }
}

__global__ void k_conv(const u16* xn, const float* K, float* yc) {
  int d = blockIdx.x;
  __shared__ float ks[L], us[L];
  for (int i = threadIdx.x; i < L; i += blockDim.x) { ks[i] = K[d * L + i]; us[i] = bf2f(xn[i * D + d]); }
  __syncthreads();
  for (int t = threadIdx.x; t < L; t += blockDim.x) {
    float acc = 0;
    for (int s = 0; s <= t; ++s) acc += ks[s] * us[t - s];
    yc[t * D + d] = acc;
  }
}

__global__ void k_gemm1(const u16* y2, const u16* w1, const u16* b1, u16* h) {
  int row = blockIdx.y, f = blockIdx.x * 256 + threadIdx.x;
  __shared__ float a[D];
  for (int i = threadIdx.x; i < D; i += 256) a[i] = bf2f(y2[row * D + i]);
  __syncthreads();
  float acc = 0;
  for (int k = 0; k < D; ++k) acc += a[k] * bf2f(w1[k * F + f]);
  float p = rbf(rbf(acc) + bf2f(b1[f]));
  float u = 0.7978845608028654f * (p + 0.044715f * p * p * p);
  h[row * F + f] = f2bf(0.5f * p * (1.f + tanhf(u)));
}

__global__ void k_gemm2(const u16* h, const u16* w2, const u16* b2, const u16* x, const u16* xn, const float* yc, const u16* Dskip, u16* out) {
  int row = blockIdx.x, d = threadIdx.x;
  __shared__ float a[F];
  for (int i = threadIdx.x; i < F; i += D) a[i] = bf2f(h[row * F + i]);
  __syncthreads();
  float acc = 0;
  for (int k = 0; k < F; ++k) acc += a[k] * bf2f(w2[k * D + d]);
  float mlp = rbf(rbf(acc) + bf2f(b2[d]));
  float ya = rbf(rbf(yc[row * D + d]) + rbf(bf2f(xn[row * D + d]) * bf2f(Dskip[d])));
  out[row * D + d] = f2bf(rbf(bf2f(x[row * D + d]) + ya) + mlp);
}

extern "C" void kernel_launch(void* const* d_in, const int* in_sizes, int n_in, void* d_out, int out_size, void* d_ws, size_t ws_size, hipStream_t stream) {
  const u16* x = (const u16*)d_in[0];
  char* ws = (char*)d_ws;
  u16* xn = (u16*)ws; u16* y2 = (u16*)(ws + (2 << 20)); float* K = (float*)(ws + (4 << 20)); float* yc = (float*)(ws + (8 << 20)); u16* h = (u16*)(ws + (12 << 20));
  k_ln<<<L, 64, 0, stream>>>(x, xn, y2);
  k_kgen<<<D, 512, 0, stream>>>((const u16*)d_in[1], (const u16*)d_in[2], (const u16*)d_in[3], (const u16*)d_in[4], (const u16*)d_in[5], (const u16*)d_in[6], (const u16*)d_in[7], K);
  k_conv<<<D, 512, 0, stream>>>(xn, K, yc);
  k_gemm1<<<dim3(F / 256, L), 256, 0, stream>>>(y2, (const u16*)d_in[10], (const u16*)d_in[11], h);
  k_gemm2<<<L, D, 0, stream>>>(h, (const u16*)d_in[12], (const u16*)d_in[13], x, xn, yc, (const u16*)d_in[8], (u16*)d_out);
}
```

```cpp
#include <hip/hip_runtime.h>
#include <stdint.h>

typedef unsigned short u16;
typedef unsigned u32;
typedef unsigned long long u64;
typedef __attribute__((ext_vector_type(4))) unsigned v4u;
typedef __attribute__((ext_vector_type(2))) unsigned v2u;
typedef __attribute__((ext_vector_type(2))) float f2;
typedef __attribute__((ext_vector_type(4))) float f32x4;
typedef __attribute__((ext_vector_type(16))) float f32x16;
typedef __attribute__((ext_vector_type(2))) __bf16 bf16x2;
typedef __attribute__((ext_vector_type(8))) __bf16 bf16x8;
typedef __attribute__((ext_vector_type(2))) _Float16 f16x2;
typedef __attribute__((ext_vector_type(8))) _Float16 f16x8;
typedef __attribute__((ext_vector_type(4))) short v4i16;
#define LAS __attribute__((address_space(3)))
#define GAS __attribute__((address_space(1)))
#define DI __device__ __forceinline__
#define RLX_AGENT __ATOMIC_RELAXED, __HIP_MEMORY_SCOPE_AGENT
#define RLX_WG __ATOMIC_RELAXED, __HIP_MEMORY_SCOPE_WORKGROUP

#if defined(__HIP_DEVICE_COMPILE__)
#pragma clang attribute push(__attribute__((target("no-packed-fp32-ops,no-fma-mix-insts,no-mad-mix-insts"))), apply_to = function)
#endif
namespace {
constexpr int SL = 2048, SD = 448, SN = 256, SF = 1792;
constexpr int NWG = 224, NTHR = 768;
constexpr u32 FLS = 128;
constexpr u32 WS_F1 = 4096, WS_F2 = 36864, WS_F3 = 151552, WS_ST = 393216, WS_XNT = 512u << 10, WS_YC = 2560u << 10, WS_HF = 4608u << 10, WS_BYTES = 12u << 20;
constexpr u32 L_XT = 0;
constexpr u32 L_UC = 0, UCS = 4160;
constexpr u32 L_KR = 33280, KRS = 4608;
constexpr u32 L_YT = 42496;
constexpr u32 L_RING = 57344;
constexpr u32 L_KG = 122880;
constexpr u32 L_ST = 155648;
constexpr u32 L_B1 = 156672, L_CS = 157696;
constexpr u32 L_B2 = 158720;
constexpr u32 L_TAG = 158976;
constexpr u32 L_SYNC = 158992;
constexpr u32 L_KM = 159072;
constexpr u32 L_KT = 159104, KTS = 1296;
constexpr u32 L_BYTES = 161696;
enum { S_XRDY = 0, S_AB = 1, S_G1 = 2, S_F1 = 3, S_KP = 4, S_CV = 6, S_KQ = 8, S_HP = 10, S_LT = 14, S_LR = 15 };
constexpr u32 SPIN_CAP = 1u << 14;

#define STAMP(n) do { } while (0)

template <typename T> DI LAS T* lp(u32 a) { return (LAS T*)(uintptr_t)a; }
DI float bf_lo(u32 v) { return __builtin_bit_cast(float, v << 16); }
DI float bf_hi(u32 v) { return __builtin_bit_cast(float, v & 0xffff0000u); }
DI u32 pk_bf(float a, float b) { bf16x2 r = {(__bf16)a, (__bf16)b}; return __builtin_bit_cast(u32, r); }
DI u32 pk_h(float a, float b) { f16x2 r = {(_Float16)a, (_Float16)b}; return __builtin_bit_cast(u32, r); }
DI float rbf(float a) { return bf_lo(pk_bf(a, a)); }
DI float bf1(u16 v) { return __builtin_bit_cast(float, (u32)v << 16); }
DI f32x16 mfma32(v4u a, v4u b, f32x16 c) { return __builtin_amdgcn_mfma_f32_32x32x16_bf16(__builtin_bit_cast(bf16x8, a), __builtin_bit_cast(bf16x8, b), c, 0, 0, 0); }
DI f32x4 mfma_h(v4u a, v4u b, f32x4 c) { return __builtin_amdgcn_mfma_f32_16x16x32_f16(__builtin_bit_cast(f16x8, a), __builtin_bit_cast(f16x8, b), c, 0, 0, 0); }
DI v2u trd(u32 a) { return __builtin_bit_cast(v2u, __builtin_amdgcn_ds_read_tr16_b64_v4i16(lp<v4i16>(a))); }
DI v4u trd2(u32 a, u32 b) { v2u x = trd(a), y = trd(b); return v4u{x.x, x.y, y.x, y.y}; }
DI float dot2(u32 a, u32 b, float c) { return __builtin_amdgcn_fdot2_f32_bf16(__builtin_bit_cast(bf16x2, a), __builtin_bit_cast(bf16x2, b), c, false); }
template <int C> DI float dpp(float v) { return __builtin_bit_cast(float, __builtin_amdgcn_update_dpp(0, __builtin_bit_cast(int, v), C, 0xf, 0xf, true)); }
DI float sum8(float v) { v += dpp<0xB1>(v); v += dpp<0x4E>(v); v += dpp<0x141>(v); return v; }
template <int C, int RM> DI float dppm(float v) { return __builtin_bit_cast(float, __builtin_amdgcn_update_dpp(0, __builtin_bit_cast(int, v), C, RM, 0xf, true)); }
DI void wsum2(float& a, float& b) {
  a += dpp<0xB1>(a); b += dpp<0xB1>(b); a += dpp<0x4E>(a); b += dpp<0x4E>(b); a += dpp<0x141>(a); b += dpp<0x141>(b); a += dpp<0x140>(a); b += dpp<0x140>(b);
  a += dppm<0x142, 0xa>(a); b += dppm<0x142, 0xa>(b); a += dppm<0x143, 0xc>(a); b += dppm<0x143, 0xc>(b);
  a = __builtin_bit_cast(float, __builtin_amdgcn_readlane(__builtin_bit_cast(int, a), 63));
  b = __builtin_bit_cast(float, __builtin_amdgcn_readlane(__builtin_bit_cast(int, b), 63));
}
DI float wmax_nn(float v) {
  v = __builtin_fmaxf(v, dpp<0xB1>(v)); v = __builtin_fmaxf(v, dpp<0x4E>(v)); v = __builtin_fmaxf(v, dpp<0x141>(v)); v = __builtin_fmaxf(v, dpp<0x140>(v));
  const int i = __builtin_bit_cast(int, v);
  const u32 a = __builtin_amdgcn_readlane(i, 0), b = __builtin_amdgcn_readlane(i, 16), c = __builtin_amdgcn_readlane(i, 32), d = __builtin_amdgcn_readlane(i, 48);
  const u32 e = a > b ? a : b, f = c > d ? c : d;
  return __builtin_bit_cast(float, e > f ? e : f);
}
DI bool wall(bool p) { return __builtin_amdgcn_ballot_w64(!p) == 0ull; }
DI bool wany(bool p) { return __builtin_amdgcn_ballot_w64(p) != 0ull; }
DI float rdl(float v, int n) { return __builtin_bit_cast(float, __builtin_amdgcn_readlane(__builtin_bit_cast(int, v), n)); }
#define WAITL() asm volatile("s_waitcnt lgkmcnt(0)" ::: "memory")
#define SB() __builtin_amdgcn_sched_barrier(0)
template <int N> DI void waitv() { asm volatile("s_waitcnt vmcnt(%0)" ::"i"(N) : "memory"); }
template <int N> DI void waitv4(v4u (&h)[4]) { asm volatile("s_waitcnt vmcnt(%4)" : "+v"(h[0]), "+v"(h[1]), "+v"(h[2]), "+v"(h[3]) : "i"(N) : "memory"); }
DI void glds(u32 m0v, u32 voff, __amdgpu_buffer_rsrc_t rs, u32 soff) {
  asm volatile("s_mov_b32 m0, %0\n\ts_nop 0\n\tbuffer_load_dwordx4 %1, %2, %3 offen lds" ::"s"(m0v), "v"(voff), "s"(rs), "s"(soff) : "memory");
}
DI void ld16_sc1(v4u& d, u32 voff, __amdgpu_buffer_rsrc_t rs, u32 soff) {
  asm volatile("buffer_load_dwordx4 %0, %1, %2, %3 offen sc1" : "=v"(d) : "v"(voff), "s"(rs), "s"(soff) : "memory");
}
DI void st16_sc1(v4u d, u32 voff, __amdgpu_buffer_rsrc_t rs, u32 soff) {
  asm volatile("buffer_store_dwordx4 %0, %1, %2, %3 offen sc1\n\ts_nop 1" ::"v"(d), "v"(voff), "s"(rs), "s"(soff) : "memory");
}
DI void lds_arrive(u32 w, int lane) { WAITL(); if (lane == 0) __hip_atomic_fetch_add(lp<u32>(L_SYNC + 4 * w), 1u, RLX_WG); }
DI void lds_wait(u32 w, u32 n) {
#pragma clang loop unroll(disable)
  for (u32 s = 0; s < (1u << 20); ++s) {
    if ((u32)__builtin_amdgcn_readfirstlane(__hip_atomic_load(lp<u32>(L_SYNC + 4 * w), RLX_WG)) >= n) break;
    __builtin_amdgcn_s_sleep(1);
  }
  asm volatile("" ::: "memory");
}
DI void poll_flags(const GAS u64* f, u64 tag) {
#pragma clang loop unroll(disable)
  for (u32 s = 0; s < SPIN_CAP; ++s) {
    const u64 v = __hip_atomic_load((GAS u64*)f, RLX_AGENT);
    if (wall(v == tag)) break;
    __builtin_amdgcn_s_sleep(1);
  }
  __builtin_amdgcn_fence(__ATOMIC_ACQUIRE, "wavefront");
  asm volatile("" ::: "memory");
}
struct cf { float x, y; };
DI cf cmul(cf a, cf b) { return cf{__builtin_fmaf(a.x, b.x, -(a.y * b.y)), __builtin_fmaf(a.x, b.y, a.y * b.x)}; }
DI cf cexp_k(float ar2, float rev, float k) {
  const float e = __builtin_amdgcn_exp2f(ar2 * k), f = __builtin_amdgcn_fractf(rev * k);
  return cf{e * __builtin_amdgcn_cosf(f), e * __builtin_amdgcn_sinf(f)};
}
DI int kpos(int tl, int r) { return tl == 0 ? (r & 7) + 128 * (r >> 3) : tl == 1 ? (r < 8 ? 256 + 128 * (r >> 2) + 2 * (r & 3) : 512 + 128 * ((r - 8) >> 1) + 4 * (r & 1)) : 1024 + 128 * r; }
DI int kidx(int tl, int r, int c) { return tl == 0 ? (r & 7) + 128 * (r >> 3) + 8 * c : tl == 1 ? (r < 8 ? 256 + 64 * (r >> 2) + (r & 3) + 4 * c : 384 + 32 * ((r - 8) >> 1) + (r & 1) + 2 * c) : 512 + 16 * r + c; }

template <class H> DI void kgen_fast(f32x4 (&acc)[3], u32 buf, int lane, float ar2, float rev, cf cb, H hook) {
  const u32 wa = buf + lane * 4;
  const u32 rb = buf + (lane & 15) * 256 + (((lane >> 4) ^ ((lane & 7) << 1)) << 4);
  auto put = [&](int row, cf v) { *lp<u32>((wa ^ ((row & 7) << 5)) + row * 256) = pk_h(v.x, v.y); };
  auto get = [&](int s) { return *lp<v4u>(rb ^ (s << 6)); };
  const cf z8 = cexp_k(ar2, rev, 8.f), z1 = cexp_k(ar2, rev, 1.f), zc = cexp_k(ar2, rev, 128.f);
  {
    const cf w{z8.x, -z8.y}, w2 = cmul(w, w);
    cf e{16384.f, 0.f}, o{w.x * 16384.f, w.y * 16384.f};
#pragma unroll
    for (int c = 0; c < 16; c += 2) { put(c, e); put(c + 1, o); if (c < 14) { e = cmul(e, w2); o = cmul(o, w2); } }
  }
  v4u ff[4];
#pragma unroll
  for (int s = 0; s < 4; ++s) ff[s] = get(s);
  cf g[8];
  g[0] = cb;
#pragma unroll
  for (int e = 1; e < 8; ++e) g[e] = cmul(g[e - 1], z1);
#pragma unroll
  for (int e = 0; e < 8; ++e) { put(e, g[e]); put(8 + e, cmul(g[e], zc)); }
#pragma unroll
  for (int s = 0; s < 4; ++s) acc[0] = mfma_h(get(s), ff[s], acc[0]);
  hook();
  const cf z256 = cmul(zc, zc), z512 = cmul(z256, z256), z1024 = cmul(z512, z512);
#pragma unroll
  for (int e = 0; e < 4; ++e) { const cf t = cmul(g[2 * e], z256); put(e, t); put(4 + e, cmul(t, zc)); }
  {
    cf a = cmul(g[0], z512), b = cmul(g[4], z512);
#pragma unroll
    for (int k = 0; k < 4; ++k) { put(8 + 2 * k, a); put(9 + 2 * k, b); if (k < 3) { a = cmul(a, zc); b = cmul(b, zc); } }
  }
#pragma unroll
  for (int s = 0; s < 4; ++s) acc[1] = mfma_h(get(s), ff[s], acc[1]);
  {
    cf a = cmul(g[0], z1024), b = cmul(a, z512);
#pragma unroll
    for (int k = 0; k < 4; ++k) { put(k, a); put(4 + k, b); a = cmul(a, zc); b = cmul(b, zc); }
    put(8, b);
  }
#pragma unroll
  for (int s = 0; s < 4; ++s) acc[2] = mfma_h(get(s), ff[s], acc[2]);
}
DI void kgen_slow(f32x4 (&acc)[3], int lane, float ar2, float rev, cf cb) {
  const int c = lane & 15, g = lane >> 4;
  for (int n = 0; n < 64; ++n) {
    const float a2 = rdl(ar2, n), rv = rdl(rev, n), cr = rdl(cb.x, n), ci = rdl(cb.y, n);
#pragma unroll
    for (int tl = 0; tl < 3; ++tl)
#pragma unroll
      for (int i = 0; i < 4; ++i) {
        const cf z = cexp_k(a2, rv, (float)(kpos(tl, 4 * g + i) + 8 * c));
        acc[tl][i] += cr * z.x - ci * z.y;
      }
  }
}
DI v4u ln_row(v4u xv, bool act, f32x4& st) {
  float xf[8], s0 = 0.f, s1 = 0.f;
#pragma unroll
  for (int k = 0; k < 4; ++k) { xf[2 * k] = bf_lo(xv[k]); xf[2 * k + 1] = bf_hi(xv[k]); s0 += xf[2 * k]; s1 += xf[2 * k + 1]; }
  s0 += s1; s1 = 0.f;
  wsum2(s0, s1);
  const float mean = s0 * (1.f / SD), m1 = rbf(mean), ms = act ? m1 : 0.f;
  float q0 = 0.f, q1 = 0.f;
#pragma unroll
  for (int k = 0; k < 4; ++k) {
    const float d0 = xf[2 * k] - ms, d1 = xf[2 * k + 1] - ms;
    q0 = __builtin_fmaf(d0, d0, q0); q1 = __builtin_fmaf(d1, d1, q1);
    xv[k] = pk_bf(d0, d1);
  }
  wsum2(q0, q1);
  const float dl = mean - m1;
  const float var = __builtin_fmaxf((q0 + q1) * (1.f / SD) - dl * dl, 0.f);
  const float r1 = __builtin_amdgcn_rcpf(rbf(__builtin_amdgcn_sqrtf(rbf(rbf(var) + 1e-5f))));
  float t1 = 0.f, t2 = 0.f;
#pragma unroll
  for (int k = 0; k < 4; ++k) {
    const u32 v = pk_bf(bf_lo(xv[k]) * r1, bf_hi(xv[k]) * r1);
    const float a0 = bf_lo(v), a1 = bf_hi(v);
    t1 += a0; t2 = __builtin_fmaf(a0, a0, t2); t1 += a1; t2 = __builtin_fmaf(a1, a1, t2);
    xv[k] = pk_h(a0, a1);
  }
  wsum2(t1, t2);
  const float mean2 = t1 * (1.f / SD), m2 = rbf(mean2);
  const float var2 = __builtin_fmaxf(t2 * (1.f / SD) - mean2 * mean2, 0.f);
  const float r2 = __builtin_amdgcn_rcpf(rbf(__builtin_amdgcn_sqrtf(rbf(rbf(var2) + 1e-5f))));
  st = f32x4{r1 * r2, -(m1 * r1 + m2) * r2, m1, r1};
  return xv;
}
DI void conv_seg(f32x4& a0, f32x4& a1, u32 ua, u32 ka, int T, int hi, int lo) {
  v4u kp[8], kc[8], uf[8];
  auto blk = [&](v4u (&kold)[8], v4u (&knew)[8], int sg) {
    const u32 u0 = ua + 512 * sg, k0 = ka - 512 * (T + 1 - sg);
#pragma unroll
    for (int s = 0; s < 8; ++s) { uf[s] = trd2(u0 + 64 * s, u0 + 64 * s + 8); knew[s] = *lp<v4u>(k0 + 64 * s); }
    if (sg <= T) {
#pragma unroll
      for (int s = 0; s < 8; ++s) a0 = mfma_h(uf[s], kold[s], a0);
    }
#pragma unroll
    for (int s = 0; s < 8; ++s) a1 = mfma_h(uf[s], knew[s], a1);
  };
#pragma unroll
  for (int s = 0; s < 8; ++s) kp[s] = *lp<v4u>(ka - 512 * (T - (hi <= T ? hi : T)) + 64 * s);
  for (int sg = hi;; sg -= 2) {
    blk(kp, kc, sg);
    if (sg - 1 < lo) break;
    blk(kc, kp, sg - 1);
    if (sg - 2 < lo) break;
  }
}
}

__global__ void __launch_bounds__(NTHR, 3)
fused_kernel(const u16* __restrict__ x, const u16* __restrict__ Are, const u16* __restrict__ Aim, const u16* __restrict__ Bre,
             const u16* __restrict__ Bim, const u16* __restrict__ Cre, const u16* __restrict__ Cim, const u16* __restrict__ logdt,
             const u16* __restrict__ Dsk, const u16* __restrict__ w1, const u16* __restrict__ b1, const u16* __restrict__ w2,
             const u16* __restrict__ b2, u16* __restrict__ out, char* __restrict__ ws) {
  __shared__ __attribute__((aligned(16))) char smem[L_BYTES];
  const int tid = threadIdx.x, lane = tid & 63, wave = __builtin_amdgcn_readfirstlane(tid >> 6);
  const int b = blockIdx.x, ti = (b & 7) * 4 + (b >> 3) / 7, tj = (b >> 3) % 7, wg = ti * 7 + tj;
  const int l15 = lane & 15, g = lane >> 4;
  STAMP(0);
  if ((u32)(uintptr_t)(LAS char*)smem != 0u) return;
  const __amdgpu_buffer_rsrc_t rws = __builtin_amdgcn_make_buffer_rsrc(ws, 0, WS_BYTES, 0x00020000);
  const __amdgpu_buffer_rsrc_t r2 = __builtin_amdgcn_make_buffer_rsrc((void*)w2, 0, SF * SD * 2, 0x00020000);

  if (wave < 4) {
    const int q = wave;
    const __amdgpu_buffer_rsrc_t rx = __builtin_amdgcn_make_buffer_rsrc((void*)x, 0, SL * SD * 2, 0x00020000);
    const __amdgpu_buffer_rsrc_t r1 = __builtin_amdgcn_make_buffer_rsrc((void*)w1, 0, SD * SF * 2, 0x00020000);
    const u32 ring = L_RING + q * 16384;
#pragma unroll
    for (int e = 0; e < 7; ++e) {
      const u32 n = e * 64 + lane, r = (n * 1171u) >> 16, c = n - r * 56;
      const u32 vo = r * 896 + ((c & ~7u) << 4);
      glds(L_XT + q * 14336 + e * 1024, vo + (((c & 7) ^ (r >> 1)) << 4), rx, (ti * 64 + q * 16) * 896);
      glds(L_XT + q * 14336 + 7168 + e * 1024, vo + (((c & 7) ^ (4 + (r >> 1))) << 4), rx, (ti * 64 + q * 16 + 8) * 896);
    }
    const u32 cs16 = ((lane & 7) ^ ((lane >> 2) & 4)) << 4;
    const u32 vo1 = (lane >> 3) * (SF * 2) + cs16, vo2 = (lane >> 3) * (SD * 2) + cs16;
    const u32 so1 = (tj * 256 + q * 64) * 2;
    const int rot = ((ti & 3) * 4 + (b & 7) * 2) % 14;
    auto stage1 = [&](int n, int e) {
      if (n < 14) {
        int s = n + rot; s = s >= 14 ? s - 14 : s;
        glds(ring + (n & 3) * 4096 + e * 1024, vo1, r1, so1 + (32 * s + 8 * e) * (SF * 2));
      } else {
        int jj = tj + ((n - 14) >> 1); jj = jj >= 7 ? jj - 7 : jj;
        glds(ring + (n & 3) * 4096 + e * 1024, vo2, r2, (jj * 256 + q * 64 + ((n - 14) & 1) * 32 + 8 * e) * (SD * 2) + tj * 128);
      }
    };
#pragma unroll
    for (int e = 0; e < 8; ++e) stage1(e >> 2, e & 3);
    __builtin_amdgcn_s_barrier();
    waitv<8>();
    lds_arrive(S_XRDY, lane);
    STAMP(1);
    lds_wait(S_LR, 7);
#pragma unroll
    for (int e = 0; e < 8; ++e) stage1(2 + (e >> 2), e & 3);
    const u32 hh = lane >> 5, q4 = l15 >> 2;
    const u32 tc = (g & 1) * 32 + (lane & 3) * 8 + ((q4 >> 1) << 6);
    const u32 ta1 = ring + (8 * hh + q4) * 128 + tc, ta2 = ring + (4 * hh + q4) * 128 + tc;
    const u32 xrow = (lane & 31) * 896, xsw = (lane >> 1) & 7;
    auto lda = [&](v4u (&a)[2], int u) {
      const u32 so = ((u >> 1) & 3) * 4096 + (u & 1) * 2048;
#pragma unroll
      for (int c = 0; c < 2; ++c) a[c] = u < 28 ? trd2((ta1 ^ (c << 6)) + so, (ta1 ^ (c << 6)) + so + 512) : trd2((ta2 ^ (c << 6)) + so, (ta2 ^ (c << 6)) + so + 1024);
    };
    auto ldx = [&](v4u (&xb)[2], int u) {
      int s = (u >> 1) + rot; s = s >= 14 ? s - 14 : s;
      const u32 a = xrow + ((((u & 3) * 2 + hh) ^ xsw) << 4) + (s >> 1) * 128;
#pragma unroll
      for (int t = 0; t < 2; ++t) xb[t] = *lp<v4u>(a + t * 32 * 896);
    };
    f32x16 acc[2][2];
#pragma unroll
    for (int c = 0; c < 2; ++c)
#pragma unroll
      for (int t = 0; t < 2; ++t)
#pragma unroll
        for (int r = 0; r < 16; ++r) acc[c][t][r] = 0.f;
    float cs0 = 0.f, cs1 = 0.f;
    v4u af[2][2], xf[2][2];
    lds_wait(S_XRDY, 4);
    STAMP(2);
    waitv<12>();
    lda(af[0], 0); ldx(xf[0], 0);
#pragma unroll
    for (int u = 0; u < 28; ++u) {
      WAITL();
      if (u & 1) waitv<8>();
      SB();
      lda(af[(u + 1) & 1], u + 1);
      if (u + 1 < 28) ldx(xf[(u + 1) & 1], u + 1);
      SB();
#pragma unroll
      for (int m = 0; m < 4; ++m) {
        acc[m >> 1][m & 1] = mfma32(af[u & 1][m >> 1], xf[u & 1][m & 1], acc[m >> 1][m & 1]);
        if (u & 1) stage1((u >> 1) + 4, m);
        if (m & 1) { cs1 = dot2(af[u & 1][1][(m >> 1) * 2], 0x3f803f80u, cs1); cs1 = dot2(af[u & 1][1][(m >> 1) * 2 + 1], 0x3f803f80u, cs1); }
        else { cs0 = dot2(af[u & 1][0][(m >> 1) * 2], 0x3f803f80u, cs0); cs0 = dot2(af[u & 1][0][(m >> 1) * 2 + 1], 0x3f803f80u, cs0); }
        asm volatile("" : "+v"(cs0), "+v"(cs1));
        SB();
      }
    }
    lds_arrive(S_G1, lane);
    {
      auto s0 = __builtin_amdgcn_permlane32_swap(__builtin_bit_cast(u32, cs0), __builtin_bit_cast(u32, cs1), false, false);
      const u32 sa = s0[0], sb = s0[1];
      *lp<float>(L_CS + (q * 64 + lane) * 4) = __builtin_bit_cast(float, sa) + __builtin_bit_cast(float, sb);
    }
    STAMP(3);
    lds_wait(S_AB, 9);
    STAMP(4);
    const u64 tag = *lp<u64>(L_TAG);
    v4u hq[4][4];
    {
      float al[2], be[2];
#pragma unroll
      for (int t = 0; t < 2; ++t) { const f2 ab = *lp<f2>(L_ST + (32 * t + (lane & 31)) * 16); al[t] = ab.x; be[t] = ab.y; }
#pragma unroll
      for (int c = 0; c < 2; ++c)
#pragma unroll
        for (int k = 0; k < 4; ++k) {
          const f32x4 bb = *lp<f32x4>(L_B1 + (64 * q + 32 * c + 8 * k + 4 * hh) * 4), cc = *lp<f32x4>(L_CS + (64 * q + 32 * c + 8 * k + 4 * hh) * 4);
#pragma unroll
          for (int t = 0; t < 2; ++t) {
            float hv[4];
#pragma unroll
            for (int i = 0; i < 4; ++i) {
              const float v = __builtin_fmaf(acc[c][t][4 * k + i], al[t], __builtin_fmaf(be[t], cc[i], bb[i]));
              const float e = __builtin_amdgcn_exp2f(v * __builtin_fmaf(v * v, -0.1029432397f, -2.302208198f));
              hv[i] = v * __builtin_amdgcn_rcpf(1.f + e);
            }
            hq[2 + c][(k >> 1) * 2 + t][(k & 1) * 2] = pk_bf(hv[0], hv[1]);
            hq[2 + c][(k >> 1) * 2 + t][(k & 1) * 2 + 1] = pk_bf(hv[2], hv[3]);
          }
        }
    }
    STAMP(5);
    const u32 hbase = WS_HF + (ti * 7 * 4 + q) * 8192;
#pragma unroll
    for (int e = 0; e < 8; ++e) st16_sc1(hq[2 + (e >> 2)][e & 3], lane * 16, rws, hbase + tj * 32768 + e * 1024);
    waitv<0>();
    if (lane == 0) __hip_atomic_store((GAS u64*)(ws + WS_F2 + (wg * 4 + q) * FLS), tag, RLX_AGENT);
    STAMP(6);
    f32x16 a2[2][2];
#pragma unroll
    for (int c = 0; c < 2; ++c)
#pragma unroll
      for (int t = 0; t < 2; ++t)
#pragma unroll
        for (int r = 0; r < 16; ++r) a2[c][t][r] = 0.f;
    const u32 fo = WS_F2 + ((ti * 7 + (lane < 7 ? lane : 0)) * 4 + q) * FLS;
    v2u fl;
    auto poll_issue = [&]() { asm volatile("buffer_load_dwordx2 %0, %1, %2, 0 offen sc1" : "=v"(fl) : "v"(fo), "s"(rws) : "memory"); };
    poll_issue();
    auto ldh1 = [&](int n, int e) {
      int jj = tj + ((n - 14) >> 1); jj = jj >= 7 ? jj - 7 : jj;
      ld16_sc1(hq[n & 3][e], lane * 16, rws, hbase + jj * 32768 + (((n - 14) & 1) * 4 + e) * 1024);
    };
    auto step2 = [&](int u) {
      const int n = u >> 1;
      SB();
      if (u + 1 < 56) lda(af[(u + 1) & 1], u + 1);
      SB();
#pragma unroll
      for (int m = 0; m < 4; ++m) {
        a2[m >> 1][m & 1] = mfma32(af[u & 1][m >> 1], hq[n & 3][(u & 1) * 2 + (m & 1)], a2[m >> 1][m & 1]);
        if ((u & 1) && n + 4 < 28) stage1(n + 4, m);
        if (!(u & 1) && u >= 34 && n + 3 < 28) ldh1(n + 3, m);
        SB();
      }
    };
#pragma unroll
    for (int u = 28; u < 32; ++u) { WAITL(); step2(u); }
    asm volatile("s_waitcnt vmcnt(8)" : "+v"(fl)::"memory");
#pragma clang loop unroll(disable)
    for (u32 sp = 0; sp < SPIN_CAP; ++sp) {
      if (wall((((u64)fl.y << 32) | fl.x) == tag)) break;
      poll_issue();
      asm volatile("s_waitcnt vmcnt(0)" : "+v"(fl)::"memory");
    }
    STAMP(7);
#pragma unroll
    for (int e = 0; e < 16; ++e) ldh1(16 + (e >> 2), e & 3);
#pragma unroll
    for (int u = 32; u < 56; ++u) {
      const int n = u >> 1;
      WAITL();
      if (!(u & 1)) {
        if (n <= 17) waitv4<12>(hq[n & 3]); else if (n == 18 || n == 25) waitv4<16>(hq[n & 3]); else if (n <= 24) waitv4<20>(hq[n & 3]);
        else if (n == 26) waitv4<8>(hq[2]); else waitv4<0>(hq[3]);
      }
      if (u >= 39 && (u & 1) && n < 27) {
        if (n <= 24) waitv<20>(); else if (n == 25) waitv<12>(); else waitv<4>();
      }
      step2(u);
    }
    STAMP(8);
#pragma unroll
    for (int c = 0; c < 2; ++c)
#pragma unroll
      for (int t = 0; t < 2; ++t)
#pragma unroll
        for (int k = 0; k < 4; ++k)
          *lp<f32x4>(ring + (32 * t + (lane & 31)) * 256 + (((8 * c + 2 * k + hh) ^ (lane & 7)) << 4)) =
              f32x4{a2[c][t][4 * k], a2[c][t][4 * k + 1], a2[c][t][4 * k + 2], a2[c][t][4 * k + 3]};
  } else {
    const int sv = wave - 4, cl = sv >> 2, sq = sv & 3, ch = 2 * wg + cl;
    const u32 kg = L_KG + sv * 4096;
    const bool two = ti % 7 == tj, act = lane < 56;
    v4u xa = {0u, 0u, 0u, 0u}, xb = xa;
    if (act) {
      xa = *(const v4u*)(x + (ti * 64 + tj * 8 + sv) * SD + lane * 8);
      if (two) xb = *(const v4u*)(x + (ti * 64 + 56 + sv) * SD + lane * 8);
    }
    const int po = ch * SN + sq * 64 + lane;
    const u16 pAr = Are[po], pAi = Aim[po], pBr = Bre[po], pBi = Bim[po], pCr = Cre[po], pCi = Cim[po];
    const float ldt = bf1(logdt[ch]);
    const u64 tag = __hip_atomic_load((GAS u64*)ws, RLX_AGENT) + 1ull;
    const u16 bv = lane < 32 ? b1[tj * 256 + sv * 32 + lane] : b2[tj * 64 + sv * 8 + (lane & 7)];
    const v4u xr = *(const v4u*)(x + (ti * 64 + lane) * SD + tj * 64 + sv * 8);
    const v4u dk = *(const v4u*)(Dsk + tj * 64 + sv * 8);
    u32 touch = 0;
    if (sv == 7 && lane < 14) touch = __hip_atomic_load(lane < 12 ? (GAS u32*)(ws + lane * (1u << 20) + 2048) : (GAS u32*)(out + (lane - 12) * (SL * SD / 2)), RLX_AGENT);
    if (sv == 0 && lane < 20) *lp<u32>(L_SYNC + 4 * lane) = 0u;
    WAITL();
    __builtin_amdgcn_s_barrier();
    {
      const u32 lt = L_RING + (sv >> 1) * 16384 + 12288 + (sv & 1) * 1088 + (sv & 2) * 64 + lane * 16;
      f32x4 st;
      const v4u ha = ln_row(xa, act, st);
      if (act) *lp<v4u>(lt) = ha;
      if (lane == 0) st16_sc1(__builtin_bit_cast(v4u, st), 0, rws, WS_ST + (ti * 64 + tj * 8 + sv) * 16);
      if (two) {
        const v4u hb = ln_row(xb, act, st);
        if (act) *lp<v4u>(lt - 4096) = hb;
        if (lane == 0) st16_sc1(__builtin_bit_cast(v4u, st), 0, rws, WS_ST + (ti * 64 + 56 + sv) * 16);
      }
      lds_arrive(S_LT, lane);
      STAMP(1);
      lds_wait(S_LT, 8);
      if (sv < 7) {
        const u32 q4 = l15 >> 2;
        const u32 a = L_RING + (q4 >> 1) * 16384 + 12288 + (q4 & 1) * 1088 + (q4 & 2) * 64 + sv * 128 + g * 32 + (lane & 3) * 8;
        st16_sc1(trd2(a, a + 32768), lane * 16, rws, WS_XNT + (ti * 8 + tj) * 7168 + sv * 1024);
        if (two) st16_sc1(trd2(a - 4096, a - 4096 + 32768), lane * 16, rws, WS_XNT + (ti * 8 + 7) * 7168 + sv * 1024);
        lds_arrive(S_LR, lane);
      }
    }
    auto publish1 = [&]() {
      waitv<0>();
      u32 old = 0;
      if (lane == 0) old = __hip_atomic_fetch_add(lp<u32>(L_SYNC + 4 * S_F1), 1u, RLX_WG);
      if (lane == 0 && old == 7) {
        __hip_atomic_store((GAS u64*)(ws + WS_F1 + (ti * 8 + tj) * FLS), tag, RLX_AGENT);
        if (two) __hip_atomic_store((GAS u64*)(ws + WS_F1 + (ti * 8 + 7) * FLS), tag, RLX_AGENT);
      }
    };
    STAMP(2);
    float kscale_inv;
    {
      const float dt = rbf(__builtin_amdgcn_exp2f(ldt * 1.4426950408889634f));
      const float bre = dt * bf1(pBr), bim = dt * bf1(pBi), cre = bf1(pCr), cim = bf1(pCi);
      const float ar2 = (dt * bf1(pAr)) * 1.4426950408889634f, rev = (dt * bf1(pAi)) * 0.15915494309189535f;
      cf cb{cre * bre - cim * bim, cre * bim + cim * bre};
      f32x4 ka[3] = {f32x4{0.f, 0.f, 0.f, 0.f}, f32x4{0.f, 0.f, 0.f, 0.f}, f32x4{0.f, 0.f, 0.f, 0.f}};
      float us0 = 1.f, us1 = 1.f;
      if (!wany(ar2 > 0.f)) {
        const float mx = wmax_nn(__builtin_fmaxf(__builtin_fabsf(cb.x), __builtin_fabsf(cb.y)));
        int eb = 267 - (int)(__builtin_bit_cast(u32, mx) >> 23);
        eb = eb > 253 ? 253 : eb;
        cb.x *= __builtin_bit_cast(float, (u32)eb << 23); cb.y *= __builtin_bit_cast(float, (u32)eb << 23);
        us0 = 6.103515625e-05f; us1 = __builtin_bit_cast(float, (u32)(254 - eb) << 23);
        kgen_fast(ka, kg, lane, ar2, rev, cb, publish1);
      } else {
        publish1();
        kgen_slow(ka, lane, ar2, rev, cb);
      }
      STAMP(3);
      float mx = 0.f;
#pragma unroll
      for (int tl = 0; tl < 3; ++tl) {
        f32x4 v = ka[tl] * us0 * us1;
        if (tl == 2) {
          if (g == 3 || (g == 2 && l15 != 0)) v[0] = 0.f;
          if (g >= 2) { v[1] = 0.f; v[2] = 0.f; v[3] = 0.f; }
        }
        *lp<f32x4>(kg + tl * 1024 + lane * 16) = v;
        mx = __builtin_fmaxf(__builtin_fmaxf(mx, __builtin_fmaxf(__builtin_fabsf(v[0]), __builtin_fabsf(v[1]))), __builtin_fmaxf(__builtin_fabsf(v[2]), __builtin_fabsf(v[3])));
      }
      mx = wmax_nn(mx);
      if (lane == 0) *lp<float>(L_KM + sv * 4) = mx;
      lds_arrive(S_KP + cl, lane);
      lds_wait(S_KP + cl, 4);
      const f32x4 m4 = *lp<f32x4>(L_KM + cl * 16);
      int eb = 268 - (int)(__builtin_bit_cast(u32, (m4[0] + m4[1]) + (m4[2] + m4[3])) >> 23);
      eb = eb > 253 ? 253 : (eb < 1 ? 1 : eb);
      const float sc = __builtin_bit_cast(float, (u32)eb << 23);
      kscale_inv = __builtin_bit_cast(float, (u32)(254 - eb) << 23);
      if (lane < 48) {
        const int it = sq * 48 + lane, tl = it >> 6, c = it & 15, gg = (it >> 4) & 3;
        const u32 pa = L_KG + cl * 16384 + it * 16;
        const f32x4 v = ((*lp<f32x4>(pa) + *lp<f32x4>(pa + 4096)) + (*lp<f32x4>(pa + 8192) + *lp<f32x4>(pa + 12288))) * sc;
#pragma unroll
        for (int i = 0; i < 4; ++i) {
          const int k = kidx(tl, 4 * gg + i, c);
          if (k <= 640) *lp<_Float16>(L_KT + cl * KTS + k * 2) = (_Float16)v[i];
        }
      }
      lds_arrive(S_KQ + cl, lane);
    }
    if (lane < 32) *lp<float>(L_B1 + (sv * 32 + lane) * 4) = bf1(bv); else if (lane < 40) *lp<float>(L_B2 + (sv * 8 + (lane & 7)) * 4) = bf1(bv);
    if (sv == 0 && lane == 0) *lp<u64>(L_TAG) = tag;
    v4u dummy = {0u, 0u, 0u, 0u};
    if (sv < 7) {
#pragma unroll
      for (int e = 0; e < 8; ++e)
        asm volatile("buffer_load_dwordx4 %0, %1, %2, %3 offen" : "+v"(dummy) : "v"(lane * 16), "s"(r2), "s"(__builtin_amdgcn_readfirstlane(((b >> 3) * 56 + sv * 8 + e) * 1024)) : "memory");
    }
    lds_arrive(S_AB, lane);
    STAMP(4);
    if (sv == 7) {
      poll_flags((GAS u64*)(ws + WS_F1 + (ti * 8 + (lane & 7)) * FLS), tag);
      *lp<v4u>(L_ST + lane * 16) = __builtin_amdgcn_raw_buffer_load_b128(rws, lane * 16, WS_ST + ti * 1024, 16);
      lds_arrive(S_AB, lane);
    }
    STAMP(5);
    lds_wait(S_KQ + cl, 4);
    v4u kr;
#pragma unroll
    for (int e = 0; e < 4; ++e) {
      const int l0 = 2047 - ((sq * 64 + lane) * 8 + 2 * e), l1 = l0 - 1;
      const u32 pb = pk_bf((float)l0, (float)l1);
      const int q0 = l0 < 128 ? l0 : (int)(pb & 0xffffu) - 17024, q1 = l1 < 128 ? l1 : (int)(pb >> 16) - 17024;
      kr[e] = (u32)*lp<u16>(L_KT + cl * KTS + q0 * 2) | ((u32)*lp<u16>(L_KT + cl * KTS + q1 * 2) << 16);
    }
    STAMP(6);
    if (b == 0 && sv == 6) {
#pragma unroll
      for (int k = 0; k < 4; ++k) poll_flags((GAS u64*)(ws + WS_F1 + (lane + 64 * k) * FLS), tag);
      if (lane == 0) __hip_atomic_store((GAS u64*)ws, tag, RLX_AGENT);
    }
    poll_flags((GAS u64*)(ws + WS_F1 + (sq * 64 + lane) * FLS), tag);
    if (sq < 3) poll_flags((GAS u64*)(ws + WS_F1 + (sq * 64 + 64) * FLS), tag);
    asm volatile("" : "+v"(dummy), "+v"(touch));
    STAMP(7);
    {
      const int m = sq * 64 + lane;
      const v4u um = __builtin_amdgcn_raw_buffer_load_b128(rws, m * 7168, WS_XNT + ch * 16, 16);
      v2u uh = __builtin_amdgcn_raw_buffer_load_b64(rws, (m < 255 ? m + 1 : 0) * 7168, WS_XNT + ch * 16, 16);
      if (m == 255) uh = v2u{0u, 0u};
      lds_wait(S_G1, 4);
      STAMP(8);
      *lp<v4u>(L_KR + cl * KRS + m * 16) = kr;
      if (sq == 3 && lane < 30) *lp<v4u>(L_KR + cl * KRS + 4096 + lane * 16) = v4u{0u, 0u, 0u, 0u};
      auto putc = [&](int mm, v4u a, v2u h) {
        const u32 o = L_UC + cl * 4 * UCS + (mm + 2) * 16;
        const u32 s0 = __builtin_amdgcn_alignbit(a.y, a.x, 16), s1 = __builtin_amdgcn_alignbit(a.z, a.y, 16), s2 = __builtin_amdgcn_alignbit(a.w, a.z, 16),
                  s3 = __builtin_amdgcn_alignbit(h.x, a.w, 16), s4 = __builtin_amdgcn_alignbit(h.y, h.x, 16);
        *lp<v4u>(o) = v4u{s0, s1, s2, s3};
        *lp<v4u>(o + UCS) = v4u{a.y, a.z, a.w, h.x};
        *lp<v4u>(o + 2 * UCS) = v4u{s1, s2, s3, s4};
        *lp<v4u>(o + 3 * UCS) = v4u{a.z, a.w, h.x, h.y};
      };
      putc(m, um, uh);
      if (m == 0) { putc(-1, v4u{0u, 0u, 0u, 0u}, v2u{um.x, um.y}); putc(-2, v4u{0u, 0u, 0u, 0u}, v2u{0u, 0u}); }
    }
    lds_arrive(S_CV + cl, lane);
    lds_wait(S_CV + cl, 4);
    STAMP(9);
    {
      const u32 ua = L_UC + (cl * 4 + (l15 >> 2)) * UCS + g * 16 + (lane & 3) * 8;
      const u32 kaddr = L_KR + cl * KRS + (2032 - 16 * l15 + 8 * g) * 2;
      f32x4 y0 = {0.f, 0.f, 0.f, 0.f}, y1 = y0;
      if (sq < 2) {
        conv_seg(y0, y1, ua, kaddr, sq ? 4 : 6, sq ? 0 : 2, 0);
        *lp<f32x4>(kg + 2048 + lane * 16) = y0; *lp<f32x4>(kg + 3072 + lane * 16) = y1;
        lds_arrive(S_HP + cl * 2 + sq, lane);
        y0 = f32x4{0.f, 0.f, 0.f, 0.f}; y1 = y0;
      }
      conv_seg(y0, y1, ua, kaddr, 2 * sq, 2 * sq + 1, sq < 2 ? 0 : (sq == 2 ? 1 : 3));
      if (sq >= 2) {
        lds_wait(S_HP + cl * 2 + 3 - sq, 1);
        const u32 hp = L_KG + (cl * 4 + 3 - sq) * 4096 + 2048 + lane * 16;
        y0 += *lp<f32x4>(hp); y1 += *lp<f32x4>(hp + 1024);
      }
      y0 *= kscale_inv; y1 *= kscale_inv;
      __builtin_amdgcn_raw_buffer_store_b64(v2u{pk_bf(y0[0], y0[1]), pk_bf(y0[2], y0[3])}, rws, (16 * l15 + 4 * g) * 2, WS_YC + ch * (SL * 2) + sq * 1024, 16);
      __builtin_amdgcn_raw_buffer_store_b64(v2u{pk_bf(y1[0], y1[1]), pk_bf(y1[2], y1[3])}, rws, (16 * l15 + 4 * g) * 2 + 512, WS_YC + ch * (SL * 2) + sq * 1024, 16);
      waitv<0>();
      if (lane == 0) __hip_atomic_store((GAS u64*)(ws + WS_F3 + (wg * 8 + sv) * FLS), tag, RLX_AGENT);
    }
    STAMP(10);
    {
      const int c0 = tj * 64 + sv * 8;
      lds_wait(S_AB, 9); lds_wait(S_KQ + (cl ^ 1), 4);
      const f2 mr = *lp<f2>(L_ST + lane * 16 + 8);
      v4u skv;
#pragma unroll
      for (int k = 0; k < 4; ++k) {
        const u32 d = pk_bf(bf_lo(xr[k]) - mr.x, bf_hi(xr[k]) - mr.x);
        const u32 xn = pk_bf(bf_lo(d) * mr.y, bf_hi(d) * mr.y);
        skv[k] = pk_bf(bf_lo(xn) * bf_lo(dk[k]), bf_hi(xn) * bf_hi(dk[k]));
      }
      poll_flags((GAS u64*)(ws + WS_F3 + ((c0 + (lane & 7)) * 4 + (ti >> 3)) * FLS), tag);
      STAMP(11);
      const u32 yt = L_YT + sv * 8 * 144;
      *lp<v4u>(yt + (lane >> 3) * 144 + (lane & 7) * 16) = __builtin_amdgcn_raw_buffer_load_b128(rws, (c0 + (lane >> 3)) * (SL * 2) + (lane & 7) * 16, WS_YC + ti * 128, 16);
      const u32 a = yt + (l15 >> 2) * 144 + g * 32 + (lane & 3) * 8;
      const v4u yv = trd2(a, a + 4 * 144);
      const u32 xo = L_KG + (lane >> 3) * 4096 + (lane & 7) * 144 + sv * 16;
      v4u r;
#pragma unroll
      for (int k = 0; k < 4; ++k) {
        const u32 ya = pk_bf(bf_lo(yv[k]) + bf_lo(skv[k]), bf_hi(yv[k]) + bf_hi(skv[k]));
        r[k] = pk_bf(bf_lo(xr[k]) + bf_lo(ya), bf_hi(xr[k]) + bf_hi(ya));
      }
      *lp<v4u>(xo) = r;
    }
    STAMP(12);
  }
  WAITL();
  __builtin_amdgcn_s_barrier();
  asm volatile("" ::: "memory");
  STAMP(13);
  if (tid < 512) {
    const int t = tid >> 3, dq = tid & 7;
    f32x4 s0 = {0.f, 0.f, 0.f, 0.f}, s1 = s0;
#pragma unroll
    for (int w = 0; w < 4; ++w) {
      s0 += *lp<f32x4>(L_RING + w * 16384 + t * 256 + (((2 * dq) ^ (t & 7)) << 4));
      s1 += *lp<f32x4>(L_RING + w * 16384 + t * 256 + (((2 * dq + 1) ^ (t & 7)) << 4));
    }
    const f32x4 c0 = *lp<f32x4>(L_B2 + dq * 32), c1 = *lp<f32x4>(L_B2 + dq * 32 + 16);
    const v4u r = *lp<v4u>(L_KG + (t >> 3) * 4096 + (t & 7) * 144 + dq * 16);
    v4u o;
#pragma unroll
    for (int k = 0; k < 4; ++k) {
      const float a = k < 2 ? s0[2 * k] : s1[2 * k - 4], c = k < 2 ? s0[2 * k + 1] : s1[2 * k - 3];
      const float ba = k < 2 ? c0[2 * k] : c1[2 * k - 4], bc = k < 2 ? c0[2 * k + 1] : c1[2 * k - 3];
      const u32 p1 = pk_bf(a, c);
      const u32 p2 = pk_bf(bf_lo(p1) + ba, bf_hi(p1) + bc);
      o[k] = pk_bf(bf_lo(r[k]) + bf_lo(p2), bf_hi(r[k]) + bf_hi(p2));
    }
    *(v4u*)(out + (ti * 64 + t) * SD + tj * 64 + dq * 8) = o;
  }
}

#if defined(__HIP_DEVICE_COMPILE__)
#pragma clang attribute pop
#endif

extern "C" void kernel_launch(void* const* d_in, const int* in_sizes, int n_in, void* d_out, int out_size, void* d_ws, size_t ws_size,
                              hipStream_t stream) {
  fused_kernel<<<NWG, NTHR, 0, stream>>>((const u16*)d_in[0], (const u16*)d_in[1], (const u16*)d_in[2], (const u16*)d_in[3], (const u16*)d_in[4],
                                         (const u16*)d_in[5], (const u16*)d_in[6], (const u16*)d_in[7], (const u16*)d_in[8], (const u16*)d_in[10],
                                         (const u16*)d_in[11], (const u16*)d_in[12], (const u16*)d_in[13], (u16*)d_out, (char*)d_ws);
}
```

```cpp
#include <hip/hip_runtime.h>
#include <stdint.h>

typedef unsigned short u16;
typedef unsigned u32;
typedef unsigned long long u64;
typedef __attribute__((ext_vector_type(4))) unsigned v4u;
typedef __attribute__((ext_vector_type(2))) unsigned v2u;
typedef __attribute__((ext_vector_type(2))) float f2;
typedef __attribute__((ext_vector_type(4))) float f32x4;
typedef __attribute__((ext_vector_type(16))) float f32x16;
typedef __attribute__((ext_vector_type(2))) __bf16 bf16x2;
typedef __attribute__((ext_vector_type(8))) __bf16 bf16x8;
typedef __attribute__((ext_vector_type(2))) _Float16 f16x2;
typedef __attribute__((ext_vector_type(8))) _Float16 f16x8;
typedef __attribute__((ext_vector_type(4))) short v4i16;
#define LAS __attribute__((address_space(3)))
#define GAS __attribute__((address_space(1)))
#define DI __device__ __forceinline__
#define RLX_AGENT __ATOMIC_RELAXED, __HIP_MEMORY_SCOPE_AGENT
#define RLX_WG __ATOMIC_RELAXED, __HIP_MEMORY_SCOPE_WORKGROUP

#if defined(__HIP_DEVICE_COMPILE__)
#pragma clang attribute push(__attribute__((target("no-packed-fp32-ops,no-fma-mix-insts,no-mad-mix-insts"))), apply_to = function)
#endif
namespace {
constexpr int SL = 2048, SD = 448, SN = 256, SF = 1792;
constexpr int NWG = 224, NTHR = 768;
constexpr u32 FLS = 128;
constexpr u32 WS_F1 = 4096, WS_F2 = 36864, WS_F3 = 151552, WS_ST = 393216, WS_XNT = 512u << 10, WS_YC = 2560u << 10, WS_HF = 4608u << 10, WS_BYTES = 12u << 20;
constexpr u32 L_XT = 0;
constexpr u32 L_UC = 0, UCS = 4160;
constexpr u32 L_KR = 33280, KRS = 4608;
constexpr u32 L_YT = 42496;
constexpr u32 L_RING = 57344;
constexpr u32 L_KG = 122880;
constexpr u32 L_ST = 155648;
constexpr u32 L_B1 = 156672, L_CS = 157696;
constexpr u32 L_B2 = 158720;
constexpr u32 L_TAG = 158976;
constexpr u32 L_SYNC = 158992;
constexpr u32 L_KM = 159072;
constexpr u32 L_KT = 159104, KTS = 1296;
constexpr u32 L_BYTES = 161696;
enum { S_XRDY = 0, S_AB = 1, S_G1 = 2, S_F1 = 3, S_KP = 4, S_CV = 6, S_KQ = 8, S_HP = 10, S_LT = 14, S_LR = 15 };
constexpr u32 SPIN_CAP = 1u << 14;

#define STAMP(n) do { } while (0)

template <typename T> DI LAS T* lp(u32 a) { return (LAS T*)(uintptr_t)a; }
DI float bf_lo(u32 v) { return __builtin_bit_cast(float, v << 16); }
DI float bf_hi(u32 v) { return __builtin_bit_cast(float, v & 0xffff0000u); }
DI u32 pk_bf(float a, float b) { bf16x2 r = {(__bf16)a, (__bf16)b}; return __builtin_bit_cast(u32, r); }
DI u32 pk_h(float a, float b) { f16x2 r = {(_Float16)a, (_Float16)b}; return __builtin_bit_cast(u32, r); }
DI float rbf(float a) { return bf_lo(pk_bf(a, a)); }
DI float bf1(u16 v) { return __builtin_bit_cast(float, (u32)v << 16); }
DI f32x16 mfma32(v4u a, v4u b, f32x16 c) { return __builtin_amdgcn_mfma_f32_32x32x16_bf16(__builtin_bit_cast(bf16x8, a), __builtin_bit_cast(bf16x8, b), c, 0, 0, 0); }
DI f32x4 mfma_h(v4u a, v4u b, f32x4 c) { return __builtin_amdgcn_mfma_f32_16x16x32_f16(__builtin_bit_cast(f16x8, a), __builtin_bit_cast(f16x8, b), c, 0, 0, 0); }
DI v2u trd(u32 a) { return __builtin_bit_cast(v2u, __builtin_amdgcn_ds_read_tr16_b64_v4i16(lp<v4i16>(a))); }
DI v4u trd2(u32 a, u32 b) { v2u x = trd(a), y = trd(b); return v4u{x.x, x.y, y.x, y.y}; }
DI float dot2(u32 a, u32 b, float c) { return __builtin_amdgcn_fdot2_f32_bf16(__builtin_bit_cast(bf16x2, a), __builtin_bit_cast(bf16x2, b), c, false); }
template <int C> DI float dpp(float v) { return __builtin_bit_cast(float, __builtin_amdgcn_update_dpp(0, __builtin_bit_cast(int, v), C, 0xf, 0xf, true)); }
DI float sum8(float v) { v += dpp<0xB1>(v); v += dpp<0x4E>(v); v += dpp<0x141>(v); return v; }
template <int C, int RM> DI float dppm(float v) { return __builtin_bit_cast(float, __builtin_amdgcn_update_dpp(0, __builtin_bit_cast(int, v), C, RM, 0xf, true)); }
DI void wsum2(float& a, float& b) {
  a += dpp<0xB1>(a); b += dpp<0xB1>(b); a += dpp<0x4E>(a); b += dpp<0x4E>(b); a += dpp<0x141>(a); b += dpp<0x141>(b); a += dpp<0x140>(a); b += dpp<0x140>(b);
  a += dppm<0x142, 0xa>(a); b += dppm<0x142, 0xa>(b); a += dppm<0x143, 0xc>(a); b += dppm<0x143, 0xc>(b);
  a = __builtin_bit_cast(float, __builtin_amdgcn_readlane(__builtin_bit_cast(int, a), 63));
  b = __builtin_bit_cast(float, __builtin_amdgcn_readlane(__builtin_bit_cast(int, b), 63));
}
DI float wmax_nn(float v) {
  v = __builtin_fmaxf(v, dpp<0xB1>(v)); v = __builtin_fmaxf(v, dpp<0x4E>(v)); v = __builtin_fmaxf(v, dpp<0x141>(v)); v = __builtin_fmaxf(v, dpp<0x140>(v));
  const int i = __builtin_bit_cast(int, v);
  const u32 a = __builtin_amdgcn_readlane(i, 0), b = __builtin_amdgcn_readlane(i, 16), c = __builtin_amdgcn_readlane(i, 32), d = __builtin_amdgcn_readlane(i, 48);
  const u32 e = a > b ? a : b, f = c > d ? c : d;
  return __builtin_bit_cast(float, e > f ? e : f);
}
DI bool wall(bool p) { return __builtin_amdgcn_ballot_w64(!p) == 0ull; }
DI bool wany(bool p) { return __builtin_amdgcn_ballot_w64(p) != 0ull; }
DI float rdl(float v, int n) { return __builtin_bit_cast(float, __builtin_amdgcn_readlane(__builtin_bit_cast(int, v), n)); }
#define WAITL() asm volatile("s_waitcnt lgkmcnt(0)" ::: "memory")
#define SB() __builtin_amdgcn_sched_barrier(0)
template <int N> DI void waitv() { asm volatile("s_waitcnt vmcnt(%0)" ::"i"(N) : "memory"); }
template <int N> DI void waitv4(v4u (&h)[4]) { asm volatile("s_waitcnt vmcnt(%4)" : "+v"(h[0]), "+v"(h[1]), "+v"(h[2]), "+v"(h[3]) : "i"(N) : "memory"); }
DI void glds(u32 m0v, u32 voff, __amdgpu_buffer_rsrc_t rs, u32 soff) {
  asm volatile("s_mov_b32 m0, %0\n\ts_nop 0\n\tbuffer_load_dwordx4 %1, %2, %3 offen lds" ::"s"(m0v), "v"(voff), "s"(rs), "s"(soff) : "memory");
}
DI void ld16_sc1(v4u& d, u32 voff, __amdgpu_buffer_rsrc_t rs, u32 soff) {
  asm volatile("buffer_load_dwordx4 %0, %1, %2, %3 offen sc1" : "=v"(d) : "v"(voff), "s"(rs), "s"(soff) : "memory");
}
DI void st16_sc1(v4u d, u32 voff, __amdgpu_buffer_rsrc_t rs, u32 soff) {
  asm volatile("buffer_store_dwordx4 %0, %1, %2, %3 offen sc1\n\ts_nop 1" ::"v"(d), "v"(voff), "s"(rs), "s"(soff) : "memory");
}
DI void lds_arrive(u32 w, int lane) { WAITL(); if (lane == 0) __hip_atomic_fetch_add(lp<u32>(L_SYNC + 4 * w), 1u, RLX_WG); }
DI void lds_wait(u32 w, u32 n) {
#pragma clang loop unroll(disable)
  for (u32 s = 0; s < (1u << 20); ++s) {
    if ((u32)__builtin_amdgcn_readfirstlane(__hip_atomic_load(lp<u32>(L_SYNC + 4 * w), RLX_WG)) >= n) break;
    __builtin_amdgcn_s_sleep(1);
  }
  asm volatile("" ::: "memory");
}
DI void poll_flags(const GAS u64* f, u64 tag) {
#pragma clang loop unroll(disable)
  for (u32 s = 0; s < SPIN_CAP; ++s) {
    const u64 v = __hip_atomic_load((GAS u64*)f, RLX_AGENT);
    if (wall(v == tag)) break;
    __builtin_amdgcn_s_sleep(1);
  }
  __builtin_amdgcn_fence(__ATOMIC_ACQUIRE, "wavefront");
  asm volatile("" ::: "memory");
}
struct cf { float x, y; };
DI cf cmul(cf a, cf b) { return cf{__builtin_fmaf(a.x, b.x, -(a.y * b.y)), __builtin_fmaf(a.x, b.y, a.y * b.x)}; }
DI cf cexp_k(float ar2, float rev, float k) {
  const float e = __builtin_amdgcn_exp2f(ar2 * k), f = __builtin_amdgcn_fractf(rev * k);
  return cf{e * __builtin_amdgcn_cosf(f), e * __builtin_amdgcn_sinf(f)};
}
DI int kpos(int tl, int r) { return tl == 0 ? (r & 7) + 128 * (r >> 3) : tl == 1 ? (r < 8 ? 256 + 128 * (r >> 2) + 2 * (r & 3) : 512 + 128 * ((r - 8) >> 1) + 4 * (r & 1)) : 1024 + 128 * r; }
DI int kidx(int tl, int r, int c) { return tl == 0 ? (r & 7) + 128 * (r >> 3) + 8 * c : tl == 1 ? (r < 8 ? 256 + 64 * (r >> 2) + (r & 3) + 4 * c : 384 + 32 * ((r - 8) >> 1) + (r & 1) + 2 * c) : 512 + 16 * r + c; }

template <class H> DI void kgen_fast(f32x4 (&acc)[3], u32 buf, int lane, float ar2, float rev, cf cb, H hook) {
  const u32 wa = buf + lane * 4;
  const u32 rb = buf + (lane & 15) * 256 + (((lane >> 4) ^ ((lane & 7) << 1)) << 4);
  auto put = [&](int row, cf v) { *lp<u32>((wa ^ ((row & 7) << 5)) + row * 256) = pk_h(v.x, v.y); };
  auto get = [&](int s) { return *lp<v4u>(rb ^ (s << 6)); };
  const cf z8 = cexp_k(ar2, rev, 8.f), z1 = cexp_k(ar2, rev, 1.f), zc = cexp_k(ar2, rev, 128.f);
  {
    const cf w{z8.x, -z8.y}, w2 = cmul(w, w);
    cf e{16384.f, 0.f}, o{w.x * 16384.f, w.y * 16384.f};
#pragma unroll
    for (int c = 0; c < 16; c += 2) { put(c, e); put(c + 1, o); if (c < 14) { e = cmul(e, w2); o = cmul(o, w2); } }
  }
  v4u ff[4];
#pragma unroll
  for (int s = 0; s < 4; ++s) ff[s] = get(s);
  cf g[8];
  g[0] = cb;
#pragma unroll
  for (int e = 1; e < 8; ++e) g[e] = cmul(g[e - 1], z1);
#pragma unroll
  for (int e = 0; e < 8; ++e) { put(e, g[e]); put(8 + e, cmul(g[e], zc)); }
#pragma unroll
  for (int s = 0; s < 4; ++s) acc[0] = mfma_h(get(s), ff[s], acc[0]);
  hook();
  const cf z256 = cmul(zc, zc), z512 = cmul(z256, z256), z1024 = cmul(z512, z512);
#pragma unroll
  for (int e = 0; e < 4; ++e) { const cf t = cmul(g[2 * e], z256); put(e, t); put(4 + e, cmul(t, zc)); }
  {
    cf a = cmul(g[0], z512), b = cmul(g[4], z512);
#pragma unroll
    for (int k = 0; k < 4; ++k) { put(8 + 2 * k, a); put(9 + 2 * k, b); if (k < 3) { a = cmul(a, zc); b = cmul(b, zc); } }
  }
#pragma unroll
  for (int s = 0; s < 4; ++s) acc[1] = mfma_h(get(s), ff[s], acc[1]);
  {
    cf a = cmul(g[0], z1024), b = cmul(a, z512);
#pragma unroll
    for (int k = 0; k < 4; ++k) { put(k, a); put(4 + k, b); a = cmul(a, zc); b = cmul(b, zc); }
    put(8, b);
  }
#pragma unroll
  for (int s = 0; s < 4; ++s) acc[2] = mfma_h(get(s), ff[s], acc[2]);
}
DI void kgen_slow(f32x4 (&acc)[3], int lane, float ar2, float rev, cf cb) {
  const int c = lane & 15, g = lane >> 4;
  for (int n = 0; n < 64; ++n) {
    const float a2 = rdl(ar2, n), rv = rdl(rev, n), cr = rdl(cb.x, n), ci = rdl(cb.y, n);
#pragma unroll
    for (int tl = 0; tl < 3; ++tl)
#pragma unroll
      for (int i = 0; i < 4; ++i) {
        const cf z = cexp_k(a2, rv, (float)(kpos(tl, 4 * g + i) + 8 * c));
        acc[tl][i] += cr * z.x - ci * z.y;
      }
  }
}
DI v4u ln_row(v4u xv, bool act, f32x4& st) {
  float xf[8], s0 = 0.f, s1 = 0.f;
#pragma unroll
  for (int k = 0; k < 4; ++k) { xf[2 * k] = bf_lo(xv[k]); xf[2 * k + 1] = bf_hi(xv[k]); s0 += xf[2 * k]; s1 += xf[2 * k + 1]; }
  s0 += s1; s1 = 0.f;
  wsum2(s0, s1);
  const float mean = s0 * (1.f / SD), m1 = rbf(mean), ms = act ? m1 : 0.f;
  float q0 = 0.f, q1 = 0.f;
#pragma unroll
  for (int k = 0; k < 4; ++k) {
    const float d0 = xf[2 * k] - ms, d1 = xf[2 * k + 1] - ms;
    q0 = __builtin_fmaf(d0, d0, q0); q1 = __builtin_fmaf(d1, d1, q1);
    xv[k] = pk_bf(d0, d1);
  }
  wsum2(q0, q1);
  const float dl = mean - m1;
  const float var = __builtin_fmaxf((q0 + q1) * (1.f / SD) - dl * dl, 0.f);
  const float r1 = __builtin_amdgcn_rcpf(rbf(__builtin_amdgcn_sqrtf(rbf(rbf(var) + 1e-5f))));
  float t1 = 0.f, t2 = 0.f;
#pragma unroll
  for (int k = 0; k < 4; ++k) {
    const u32 v = pk_bf(bf_lo(xv[k]) * r1, bf_hi(xv[k]) * r1);
    const float a0 = bf_lo(v), a1 = bf_hi(v);
    t1 += a0; t2 = __builtin_fmaf(a0, a0, t2); t1 += a1; t2 = __builtin_fmaf(a1, a1, t2);
    xv[k] = pk_h(a0, a1);
  }
  wsum2(t1, t2);
  const float mean2 = t1 * (1.f / SD), m2 = rbf(mean2);
  const float var2 = __builtin_fmaxf(t2 * (1.f / SD) - mean2 * mean2, 0.f);
  const float r2 = __builtin_amdgcn_rcpf(rbf(__builtin_amdgcn_sqrtf(rbf(rbf(var2) + 1e-5f))));
  st = f32x4{r1 * r2, -(m1 * r1 + m2) * r2, m1, r1};
  return xv;
}
DI void conv_seg(f32x4& a0, f32x4& a1, u32 ua, u32 ka, int T, int hi, int lo) {
  v4u kp[8], kc[8], uf[8];
  auto blk = [&](v4u (&kold)[8], v4u (&knew)[8], int sg) {
    const u32 u0 = ua + 512 * sg, k0 = ka - 512 * (T + 1 - sg);
#pragma unroll
    for (int s = 0; s < 8; ++s) { uf[s] = trd2(u0 + 64 * s, u0 + 64 * s + 8); knew[s] = *lp<v4u>(k0 + 64 * s); }
    if (sg <= T) {
#pragma unroll
      for (int s = 0; s < 8; ++s) a0 = mfma_h(uf[s], kold[s], a0);
    }
#pragma unroll
    for (int s = 0; s < 8; ++s) a1 = mfma_h(uf[s], knew[s], a1);
  };
#pragma unroll
  for (int s = 0; s < 8; ++s) kp[s] = *lp<v4u>(ka - 512 * (T - (hi <= T ? hi : T)) + 64 * s);
  for (int sg = hi;; sg -= 2) {
    blk(kp, kc, sg);
    if (sg - 1 < lo) break;
    blk(kc, kp, sg - 1);
    if (sg - 2 < lo) break;
  }
}
}

__global__ void __launch_bounds__(NTHR, 3)
fused_kernel(const u16* __restrict__ x, const u16* __restrict__ Are, const u16* __restrict__ Aim, const u16* __restrict__ Bre,
             const u16* __restrict__ Bim, const u16* __restrict__ Cre, const u16* __restrict__ Cim, const u16* __restrict__ logdt,
             const u16* __restrict__ Dsk, const u16* __restrict__ w1, const u16* __restrict__ b1, const u16* __restrict__ w2,
             const u16* __restrict__ b2, u16* __restrict__ out, char* __restrict__ ws) {
  __shared__ __attribute__((aligned(16))) char smem[L_BYTES];
  const int tid = threadIdx.x, lane = tid & 63, wave = __builtin_amdgcn_readfirstlane(tid >> 6);
  const int b = blockIdx.x, ti = (b & 7) * 4 + (b >> 3) / 7, tj = (b >> 3) % 7, wg = ti * 7 + tj;
  const int l15 = lane & 15, g = lane >> 4;
  STAMP(0);
  if ((u32)(uintptr_t)(LAS char*)smem != 0u) return;
  u32 cpf = 0;
  if (wave == 11) {
    u64 pc; u32 len;
    asm volatile("s_getpc_b64 %0\nLcode_beg_%=:\n\ts_mov_b32 %1, Lcode_end-Lcode_beg_%=" : "=s"(pc), "=s"(len));
    const u32 o = ((b >> 3) * 16 + (lane & 15)) * 128 + (lane >> 4) * (28 * 2048);
    if (o < len) cpf = *(const GAS u32*)(pc + o);
  }
  const __amdgpu_buffer_rsrc_t rws = __builtin_amdgcn_make_buffer_rsrc(ws, 0, WS_BYTES, 0x00020000);
  const __amdgpu_buffer_rsrc_t r2 = __builtin_amdgcn_make_buffer_rsrc((void*)w2, 0, SF * SD * 2, 0x00020000);

  if (wave < 4) {
    const int q = wave;
    const __amdgpu_buffer_rsrc_t rx = __builtin_amdgcn_make_buffer_rsrc((void*)x, 0, SL * SD * 2, 0x00020000);
    const __amdgpu_buffer_rsrc_t r1 = __builtin_amdgcn_make_buffer_rsrc((void*)w1, 0, SD * SF * 2, 0x00020000);
    const u32 ring = L_RING + q * 16384;
#pragma unroll
    for (int e = 0; e < 7; ++e) {
      const u32 n = e * 64 + lane, r = (n * 1171u) >> 16, c = n - r * 56;
      const u32 vo = r * 896 + ((c & ~7u) << 4);
      glds(L_XT + q * 14336 + e * 1024, vo + (((c & 7) ^ (r >> 1)) << 4), rx, (ti * 64 + q * 16) * 896);
      glds(L_XT + q * 14336 + 7168 + e * 1024, vo + (((c & 7) ^ (4 + (r >> 1))) << 4), rx, (ti * 64 + q * 16 + 8) * 896);
    }
    const u32 cs16 = ((lane & 7) ^ ((lane >> 2) & 4)) << 4;
    const u32 vo1 = (lane >> 3) * (SF * 2) + cs16, vo2 = (lane >> 3) * (SD * 2) + cs16;
    const u32 so1 = (tj * 256 + q * 64) * 2;
    const int rot = ((ti & 3) * 4 + (b & 7) * 2) % 14;
    auto stage1 = [&](int n, int e) {
      if (n < 14) {
        int s = n + rot; s = s >= 14 ? s - 14 : s;
        glds(ring + (n & 3) * 4096 + e * 1024, vo1, r1, so1 + (32 * s + 8 * e) * (SF * 2));
      } else {
        int jj = tj + ((n - 14) >> 1); jj = jj >= 7 ? jj - 7 : jj;
        glds(ring + (n & 3) * 4096 + e * 1024, vo2, r2, (jj * 256 + q * 64 + ((n - 14) & 1) * 32 + 8 * e) * (SD * 2) + tj * 128);
      }
    };
#pragma unroll
    for (int e = 0; e < 8; ++e) stage1(e >> 2, e & 3);
    __builtin_amdgcn_s_barrier();
    waitv<8>();
    lds_arrive(S_XRDY, lane);
    STAMP(1);
    lds_wait(S_LR, 7);
#pragma unroll
    for (int e = 0; e < 8; ++e) stage1(2 + (e >> 2), e & 3);
    const u32 hh = lane >> 5, q4 = l15 >> 2;
    const u32 tc = (g & 1) * 32 + (lane & 3) * 8 + ((q4 >> 1) << 6);
    const u32 ta1 = ring + (8 * hh + q4) * 128 + tc, ta2 = ring + (4 * hh + q4) * 128 + tc;
    const u32 xrow = (lane & 31) * 896, xsw = (lane >> 1) & 7;
    auto lda = [&](v4u (&a)[2], int u) {
      const u32 so = ((u >> 1) & 3) * 4096 + (u & 1) * 2048;
#pragma unroll
      for (int c = 0; c < 2; ++c) a[c] = u < 28 ? trd2((ta1 ^ (c << 6)) + so, (ta1 ^ (c << 6)) + so + 512) : trd2((ta2 ^ (c << 6)) + so, (ta2 ^ (c << 6)) + so + 1024);
    };
    auto ldx = [&](v4u (&xb)[2], int u) {
      int s = (u >> 1) + rot; s = s >= 14 ? s - 14 : s;
      const u32 a = xrow + ((((u & 3) * 2 + hh) ^ xsw) << 4) + (s >> 1) * 128;
#pragma unroll
      for (int t = 0; t < 2; ++t) xb[t] = *lp<v4u>(a + t * 32 * 896);
    };
    f32x16 acc[2][2];
#pragma unroll
    for (int c = 0; c < 2; ++c)
#pragma unroll
      for (int t = 0; t < 2; ++t)
#pragma unroll
        for (int r = 0; r < 16; ++r) acc[c][t][r] = 0.f;
    float cs0 = 0.f, cs1 = 0.f;
    v4u af[2][2], xf[2][2];
    lds_wait(S_XRDY, 4);
    STAMP(2);
    waitv<12>();
    lda(af[0], 0); ldx(xf[0], 0);
#pragma unroll
    for (int u = 0; u < 28; ++u) {
      WAITL();
      if (u & 1) waitv<8>();
      SB();
      lda(af[(u + 1) & 1], u + 1);
      if (u + 1 < 28) ldx(xf[(u + 1) & 1], u + 1);
      SB();
#pragma unroll
      for (int m = 0; m < 4; ++m) {
        acc[m >> 1][m & 1] = mfma32(af[u & 1][m >> 1], xf[u & 1][m & 1], acc[m >> 1][m & 1]);
        if (u & 1) stage1((u >> 1) + 4, m);
        if (m & 1) { cs1 = dot2(af[u & 1][1][(m >> 1) * 2], 0x3f803f80u, cs1); cs1 = dot2(af[u & 1][1][(m >> 1) * 2 + 1], 0x3f803f80u, cs1); }
        else { cs0 = dot2(af[u & 1][0][(m >> 1) * 2], 0x3f803f80u, cs0); cs0 = dot2(af[u & 1][0][(m >> 1) * 2 + 1], 0x3f803f80u, cs0); }
        asm volatile("" : "+v"(cs0), "+v"(cs1));
        SB();
      }
    }
    lds_arrive(S_G1, lane);
    {
      auto s0 = __builtin_amdgcn_permlane32_swap(__builtin_bit_cast(u32, cs0), __builtin_bit_cast(u32, cs1), false, false);
      const u32 sa = s0[0], sb = s0[1];
      *lp<float>(L_CS + (q * 64 + lane) * 4) = __builtin_bit_cast(float, sa) + __builtin_bit_cast(float, sb);
    }
    STAMP(3);
    lds_wait(S_AB, 9);
    STAMP(4);
    const u64 tag = *lp<u64>(L_TAG);
    v4u hq[4][4];
    {
      float al[2], be[2];
#pragma unroll
      for (int t = 0; t < 2; ++t) { const f2 ab = *lp<f2>(L_ST + (32 * t + (lane & 31)) * 16); al[t] = ab.x; be[t] = ab.y; }
#pragma unroll
      for (int c = 0; c < 2; ++c)
#pragma unroll
        for (int k = 0; k < 4; ++k) {
          const f32x4 bb = *lp<f32x4>(L_B1 + (64 * q + 32 * c + 8 * k + 4 * hh) * 4), cc = *lp<f32x4>(L_CS + (64 * q + 32 * c + 8 * k + 4 * hh) * 4);
#pragma unroll
          for (int t = 0; t < 2; ++t) {
            float hv[4];
#pragma unroll
            for (int i = 0; i < 4; ++i) {
              const float v = __builtin_fmaf(acc[c][t][4 * k + i], al[t], __builtin_fmaf(be[t], cc[i], bb[i]));
              const float e = __builtin_amdgcn_exp2f(v * __builtin_fmaf(v * v, -0.1029432397f, -2.302208198f));
              hv[i] = v * __builtin_amdgcn_rcpf(1.f + e);
            }
            hq[2 + c][(k >> 1) * 2 + t][(k & 1) * 2] = pk_bf(hv[0], hv[1]);
            hq[2 + c][(k >> 1) * 2 + t][(k & 1) * 2 + 1] = pk_bf(hv[2], hv[3]);
          }
        }
    }
    STAMP(5);
    const u32 hbase = WS_HF + (ti * 7 * 4 + q) * 8192;
#pragma unroll
    for (int e = 0; e < 8; ++e) st16_sc1(hq[2 + (e >> 2)][e & 3], lane * 16, rws, hbase + tj * 32768 + e * 1024);
    waitv<0>();
    if (lane == 0) __hip_atomic_store((GAS u64*)(ws + WS_F2 + (wg * 4 + q) * FLS), tag, RLX_AGENT);
    STAMP(6);
    f32x16 a2[2][2];
#pragma unroll
    for (int c = 0; c < 2; ++c)
#pragma unroll
      for (int t = 0; t < 2; ++t)
#pragma unroll
        for (int r = 0; r < 16; ++r) a2[c][t][r] = 0.f;
    const u32 fo = WS_F2 + ((ti * 7 + (lane < 7 ? lane : 0)) * 4 + q) * FLS;
    v2u fl;
    auto poll_issue = [&]() { asm volatile("buffer_load_dwordx2 %0, %1, %2, 0 offen sc1" : "=v"(fl) : "v"(fo), "s"(rws) : "memory"); };
    poll_issue();
    auto ldh1 = [&](int n, int e) {
      int jj = tj + ((n - 14) >> 1); jj = jj >= 7 ? jj - 7 : jj;
      ld16_sc1(hq[n & 3][e], lane * 16, rws, hbase + jj * 32768 + (((n - 14) & 1) * 4 + e) * 1024);
    };
    auto step2 = [&](int u) {
      const int n = u >> 1;
      SB();
      if (u + 1 < 56) lda(af[(u + 1) & 1], u + 1);
      SB();
#pragma unroll
      for (int m = 0; m < 4; ++m) {
        a2[m >> 1][m & 1] = mfma32(af[u & 1][m >> 1], hq[n & 3][(u & 1) * 2 + (m & 1)], a2[m >> 1][m & 1]);
        if ((u & 1) && n + 4 < 28) stage1(n + 4, m);
        if (!(u & 1) && u >= 34 && n + 3 < 28) ldh1(n + 3, m);
        SB();
      }
    };
#pragma unroll
    for (int u = 28; u < 32; ++u) { WAITL(); step2(u); }
    asm volatile("s_waitcnt vmcnt(8)" : "+v"(fl)::"memory");
#pragma clang loop unroll(disable)
    for (u32 sp = 0; sp < SPIN_CAP; ++sp) {
      if (wall((((u64)fl.y << 32) | fl.x) == tag)) break;
      poll_issue();
      asm volatile("s_waitcnt vmcnt(0)" : "+v"(fl)::"memory");
    }
    STAMP(7);
#pragma unroll
    for (int e = 0; e < 16; ++e) ldh1(16 + (e >> 2), e & 3);
#pragma unroll
    for (int u = 32; u < 56; ++u) {
      const int n = u >> 1;
      WAITL();
      if (!(u & 1)) {
        if (n <= 17) waitv4<12>(hq[n & 3]); else if (n == 18 || n == 25) waitv4<16>(hq[n & 3]); else if (n <= 24) waitv4<20>(hq[n & 3]);
        else if (n == 26) waitv4<8>(hq[2]); else waitv4<0>(hq[3]);
      }
      if (u >= 39 && (u & 1) && n < 27) {
        if (n <= 24) waitv<20>(); else if (n == 25) waitv<12>(); else waitv<4>();
      }
      step2(u);
    }
    STAMP(8);
#pragma unroll
    for (int c = 0; c < 2; ++c)
#pragma unroll
      for (int t = 0; t < 2; ++t)
#pragma unroll
        for (int k = 0; k < 4; ++k)
          *lp<f32x4>(ring + (32 * t + (lane & 31)) * 256 + (((8 * c + 2 * k + hh) ^ (lane & 7)) << 4)) =
              f32x4{a2[c][t][4 * k], a2[c][t][4 * k + 1], a2[c][t][4 * k + 2], a2[c][t][4 * k + 3]};
  } else {
    const int sv = wave - 4, cl = sv >> 2, sq = sv & 3, ch = 2 * wg + cl;
    const u32 kg = L_KG + sv * 4096;
    const bool two = ti % 7 == tj, act = lane < 56;
    v4u xa = {0u, 0u, 0u, 0u}, xb = xa;
    if (act) {
      xa = *(const v4u*)(x + (ti * 64 + tj * 8 + sv) * SD + lane * 8);
      if (two) xb = *(const v4u*)(x + (ti * 64 + 56 + sv) * SD + lane * 8);
    }
    const int po = ch * SN + sq * 64 + lane;
    const u16 pAr = Are[po], pAi = Aim[po], pBr = Bre[po], pBi = Bim[po], pCr = Cre[po], pCi = Cim[po];
    const float ldt = bf1(logdt[ch]);
    const u64 tag = __hip_atomic_load((GAS u64*)ws, RLX_AGENT) + 1ull;
    const u16 bv = lane < 32 ? b1[tj * 256 + sv * 32 + lane] : b2[tj * 64 + sv * 8 + (lane & 7)];
    const v4u xr = *(const v4u*)(x + (ti * 64 + lane) * SD + tj * 64 + sv * 8);
    const v4u dk = *(const v4u*)(Dsk + tj * 64 + sv * 8);
    u32 touch = 0;
    if (sv == 7 && lane < 14) touch = __hip_atomic_load(lane < 12 ? (GAS u32*)(ws + lane * (1u << 20) + 2048) : (GAS u32*)(out + (lane - 12) * (SL * SD / 2)), RLX_AGENT);
    if (sv == 0 && lane < 20) *lp<u32>(L_SYNC + 4 * lane) = 0u;
    WAITL();
    __builtin_amdgcn_s_barrier();
    {
      const u32 lt = L_RING + (sv >> 1) * 16384 + 12288 + (sv & 1) * 1088 + (sv & 2) * 64 + lane * 16;
      f32x4 st;
      const v4u ha = ln_row(xa, act, st);
      if (act) *lp<v4u>(lt) = ha;
      if (lane == 0) st16_sc1(__builtin_bit_cast(v4u, st), 0, rws, WS_ST + (ti * 64 + tj * 8 + sv) * 16);
      if (two) {
        const v4u hb = ln_row(xb, act, st);
        if (act) *lp<v4u>(lt - 4096) = hb;
        if (lane == 0) st16_sc1(__builtin_bit_cast(v4u, st), 0, rws, WS_ST + (ti * 64 + 56 + sv) * 16);
      }
      lds_arrive(S_LT, lane);
      STAMP(1);
      lds_wait(S_LT, 8);
      if (sv < 7) {
        const u32 q4 = l15 >> 2;
        const u32 a = L_RING + (q4 >> 1) * 16384 + 12288 + (q4 & 1) * 1088 + (q4 & 2) * 64 + sv * 128 + g * 32 + (lane & 3) * 8;
        st16_sc1(trd2(a, a + 32768), lane * 16, rws, WS_XNT + (ti * 8 + tj) * 7168 + sv * 1024);
        if (two) st16_sc1(trd2(a - 4096, a - 4096 + 32768), lane * 16, rws, WS_XNT + (ti * 8 + 7) * 7168 + sv * 1024);
        lds_arrive(S_LR, lane);
      }
    }
    auto publish1 = [&]() {
      waitv<0>();
      u32 old = 0;
      if (lane == 0) old = __hip_atomic_fetch_add(lp<u32>(L_SYNC + 4 * S_F1), 1u, RLX_WG);
      if (lane == 0 && old == 7) {
        __hip_atomic_store((GAS u64*)(ws + WS_F1 + (ti * 8 + tj) * FLS), tag, RLX_AGENT);
        if (two) __hip_atomic_store((GAS u64*)(ws + WS_F1 + (ti * 8 + 7) * FLS), tag, RLX_AGENT);
      }
    };
    STAMP(2);
    float kscale_inv;
    {
      const float dt = rbf(__builtin_amdgcn_exp2f(ldt * 1.4426950408889634f));
      const float bre = dt * bf1(pBr), bim = dt * bf1(pBi), cre = bf1(pCr), cim = bf1(pCi);
      const float ar2 = (dt * bf1(pAr)) * 1.4426950408889634f, rev = (dt * bf1(pAi)) * 0.15915494309189535f;
      cf cb{cre * bre - cim * bim, cre * bim + cim * bre};
      f32x4 ka[3] = {f32x4{0.f, 0.f, 0.f, 0.f}, f32x4{0.f, 0.f, 0.f, 0.f}, f32x4{0.f, 0.f, 0.f, 0.f}};
      float us0 = 1.f, us1 = 1.f;
      if (!wany(ar2 > 0.f)) {
        const float mx = wmax_nn(__builtin_fmaxf(__builtin_fabsf(cb.x), __builtin_fabsf(cb.y)));
        int eb = 267 - (int)(__builtin_bit_cast(u32, mx) >> 23);
        eb = eb > 253 ? 253 : eb;
        cb.x *= __builtin_bit_cast(float, (u32)eb << 23); cb.y *= __builtin_bit_cast(float, (u32)eb << 23);
        us0 = 6.103515625e-05f; us1 = __builtin_bit_cast(float, (u32)(254 - eb) << 23);
        kgen_fast(ka, kg, lane, ar2, rev, cb, publish1);
      } else {
        publish1();
        kgen_slow(ka, lane, ar2, rev, cb);
      }
      STAMP(3);
      float mx = 0.f;
#pragma unroll
      for (int tl = 0; tl < 3; ++tl) {
        f32x4 v = ka[tl] * us0 * us1;
        if (tl == 2) {
          if (g == 3 || (g == 2 && l15 != 0)) v[0] = 0.f;
          if (g >= 2) { v[1] = 0.f; v[2] = 0.f; v[3] = 0.f; }
        }
        *lp<f32x4>(kg + tl * 1024 + lane * 16) = v;
        mx = __builtin_fmaxf(__builtin_fmaxf(mx, __builtin_fmaxf(__builtin_fabsf(v[0]), __builtin_fabsf(v[1]))), __builtin_fmaxf(__builtin_fabsf(v[2]), __builtin_fabsf(v[3])));
      }
      mx = wmax_nn(mx);
      if (lane == 0) *lp<float>(L_KM + sv * 4) = mx;
      lds_arrive(S_KP + cl, lane);
      lds_wait(S_KP + cl, 4);
      const f32x4 m4 = *lp<f32x4>(L_KM + cl * 16);
      int eb = 268 - (int)(__builtin_bit_cast(u32, (m4[0] + m4[1]) + (m4[2] + m4[3])) >> 23);
      eb = eb > 253 ? 253 : (eb < 1 ? 1 : eb);
      const float sc = __builtin_bit_cast(float, (u32)eb << 23);
      kscale_inv = __builtin_bit_cast(float, (u32)(254 - eb) << 23);
      if (lane < 48) {
        const int it = sq * 48 + lane, tl = it >> 6, c = it & 15, gg = (it >> 4) & 3;
        const u32 pa = L_KG + cl * 16384 + it * 16;
        const f32x4 v = ((*lp<f32x4>(pa) + *lp<f32x4>(pa + 4096)) + (*lp<f32x4>(pa + 8192) + *lp<f32x4>(pa + 12288))) * sc;
#pragma unroll
        for (int i = 0; i < 4; ++i) {
          const int k = kidx(tl, 4 * gg + i, c);
          if (k <= 640) *lp<_Float16>(L_KT + cl * KTS + k * 2) = (_Float16)v[i];
        }
      }
      lds_arrive(S_KQ + cl, lane);
    }
    if (lane < 32) *lp<float>(L_B1 + (sv * 32 + lane) * 4) = bf1(bv); else if (lane < 40) *lp<float>(L_B2 + (sv * 8 + (lane & 7)) * 4) = bf1(bv);
    if (sv == 0 && lane == 0) *lp<u64>(L_TAG) = tag;
    v4u dummy = {0u, 0u, 0u, 0u};
    if (sv < 7) {
#pragma unroll
      for (int e = 0; e < 8; ++e)
        asm volatile("buffer_load_dwordx4 %0, %1, %2, %3 offen" : "+v"(dummy) : "v"(lane * 16), "s"(r2), "s"(__builtin_amdgcn_readfirstlane(((b >> 3) * 56 + sv * 8 + e) * 1024)) : "memory");
    }
    lds_arrive(S_AB, lane);
    STAMP(4);
    if (sv == 7) {
      poll_flags((GAS u64*)(ws + WS_F1 + (ti * 8 + (lane & 7)) * FLS), tag);
      *lp<v4u>(L_ST + lane * 16) = __builtin_amdgcn_raw_buffer_load_b128(rws, lane * 16, WS_ST + ti * 1024, 16);
      lds_arrive(S_AB, lane);
    }
    STAMP(5);
    lds_wait(S_KQ + cl, 4);
    v4u kr;
#pragma unroll
    for (int e = 0; e < 4; ++e) {
      const int l0 = 2047 - ((sq * 64 + lane) * 8 + 2 * e), l1 = l0 - 1;
      const u32 pb = pk_bf((float)l0, (float)l1);
      const int q0 = l0 < 128 ? l0 : (int)(pb & 0xffffu) - 17024, q1 = l1 < 128 ? l1 : (int)(pb >> 16) - 17024;
      kr[e] = (u32)*lp<u16>(L_KT + cl * KTS + q0 * 2) | ((u32)*lp<u16>(L_KT + cl * KTS + q1 * 2) << 16);
    }
    STAMP(6);
    if (b == 0 && sv == 6) {
#pragma unroll
      for (int k = 0; k < 4; ++k) poll_flags((GAS u64*)(ws + WS_F1 + (lane + 64 * k) * FLS), tag);
      if (lane == 0) __hip_atomic_store((GAS u64*)ws, tag, RLX_AGENT);
    }
    poll_flags((GAS u64*)(ws + WS_F1 + (sq * 64 + lane) * FLS), tag);
    if (sq < 3) poll_flags((GAS u64*)(ws + WS_F1 + (sq * 64 + 64) * FLS), tag);
    asm volatile("" : "+v"(dummy), "+v"(touch));
    STAMP(7);
    {
      const int m = sq * 64 + lane;
      const v4u um = __builtin_amdgcn_raw_buffer_load_b128(rws, m * 7168, WS_XNT + ch * 16, 16);
      v2u uh = __builtin_amdgcn_raw_buffer_load_b64(rws, (m < 255 ? m + 1 : 0) * 7168, WS_XNT + ch * 16, 16);
      if (m == 255) uh = v2u{0u, 0u};
      lds_wait(S_G1, 4);
      STAMP(8);
      *lp<v4u>(L_KR + cl * KRS + m * 16) = kr;
      if (sq == 3 && lane < 30) *lp<v4u>(L_KR + cl * KRS + 4096 + lane * 16) = v4u{0u, 0u, 0u, 0u};
      auto putc = [&](int mm, v4u a, v2u h) {
        const u32 o = L_UC + cl * 4 * UCS + (mm + 2) * 16;
        const u32 s0 = __builtin_amdgcn_alignbit(a.y, a.x, 16), s1 = __builtin_amdgcn_alignbit(a.z, a.y, 16), s2 = __builtin_amdgcn_alignbit(a.w, a.z, 16),
                  s3 = __builtin_amdgcn_alignbit(h.x, a.w, 16), s4 = __builtin_amdgcn_alignbit(h.y, h.x, 16);
        *lp<v4u>(o) = v4u{s0, s1, s2, s3};
        *lp<v4u>(o + UCS) = v4u{a.y, a.z, a.w, h.x};
        *lp<v4u>(o + 2 * UCS) = v4u{s1, s2, s3, s4};
        *lp<v4u>(o + 3 * UCS) = v4u{a.z, a.w, h.x, h.y};
      };
      putc(m, um, uh);
      if (m == 0) { putc(-1, v4u{0u, 0u, 0u, 0u}, v2u{um.x, um.y}); putc(-2, v4u{0u, 0u, 0u, 0u}, v2u{0u, 0u}); }
    }
    lds_arrive(S_CV + cl, lane);
    lds_wait(S_CV + cl, 4);
    STAMP(9);
    {
      const u32 ua = L_UC + (cl * 4 + (l15 >> 2)) * UCS + g * 16 + (lane & 3) * 8;
      const u32 kaddr = L_KR + cl * KRS + (2032 - 16 * l15 + 8 * g) * 2;
      f32x4 y0 = {0.f, 0.f, 0.f, 0.f}, y1 = y0;
      if (sq < 2) {
        conv_seg(y0, y1, ua, kaddr, sq ? 4 : 6, sq ? 0 : 2, 0);
        *lp<f32x4>(kg + 2048 + lane * 16) = y0; *lp<f32x4>(kg + 3072 + lane * 16) = y1;
        lds_arrive(S_HP + cl * 2 + sq, lane);
        y0 = f32x4{0.f, 0.f, 0.f, 0.f}; y1 = y0;
      }
      conv_seg(y0, y1, ua, kaddr, 2 * sq, 2 * sq + 1, sq < 2 ? 0 : (sq == 2 ? 1 : 3));
      if (sq >= 2) {
        lds_wait(S_HP + cl * 2 + 3 - sq, 1);
        const u32 hp = L_KG + (cl * 4 + 3 - sq) * 4096 + 2048 + lane * 16;
        y0 += *lp<f32x4>(hp); y1 += *lp<f32x4>(hp + 1024);
      }
      y0 *= kscale_inv; y1 *= kscale_inv;
      __builtin_amdgcn_raw_buffer_store_b64(v2u{pk_bf(y0[0], y0[1]), pk_bf(y0[2], y0[3])}, rws, (16 * l15 + 4 * g) * 2, WS_YC + ch * (SL * 2) + sq * 1024, 16);
      __builtin_amdgcn_raw_buffer_store_b64(v2u{pk_bf(y1[0], y1[1]), pk_bf(y1[2], y1[3])}, rws, (16 * l15 + 4 * g) * 2 + 512, WS_YC + ch * (SL * 2) + sq * 1024, 16);
      waitv<0>();
      if (lane == 0) __hip_atomic_store((GAS u64*)(ws + WS_F3 + (wg * 8 + sv) * FLS), tag, RLX_AGENT);
    }
    STAMP(10);
    {
      const int c0 = tj * 64 + sv * 8;
      lds_wait(S_AB, 9); lds_wait(S_KQ + (cl ^ 1), 4);
      const f2 mr = *lp<f2>(L_ST + lane * 16 + 8);
      v4u skv;
#pragma unroll
      for (int k = 0; k < 4; ++k) {
        const u32 d = pk_bf(bf_lo(xr[k]) - mr.x, bf_hi(xr[k]) - mr.x);
        const u32 xn = pk_bf(bf_lo(d) * mr.y, bf_hi(d) * mr.y);
        skv[k] = pk_bf(bf_lo(xn) * bf_lo(dk[k]), bf_hi(xn) * bf_hi(dk[k]));
      }
      poll_flags((GAS u64*)(ws + WS_F3 + ((c0 + (lane & 7)) * 4 + (ti >> 3)) * FLS), tag);
      STAMP(11);
      const u32 yt = L_YT + sv * 8 * 144;
      *lp<v4u>(yt + (lane >> 3) * 144 + (lane & 7) * 16) = __builtin_amdgcn_raw_buffer_load_b128(rws, (c0 + (lane >> 3)) * (SL * 2) + (lane & 7) * 16, WS_YC + ti * 128, 16);
      const u32 a = yt + (l15 >> 2) * 144 + g * 32 + (lane & 3) * 8;
      const v4u yv = trd2(a, a + 4 * 144);
      const u32 xo = L_KG + (lane >> 3) * 4096 + (lane & 7) * 144 + sv * 16;
      v4u r;
#pragma unroll
      for (int k = 0; k < 4; ++k) {
        const u32 ya = pk_bf(bf_lo(yv[k]) + bf_lo(skv[k]), bf_hi(yv[k]) + bf_hi(skv[k]));
        r[k] = pk_bf(bf_lo(xr[k]) + bf_lo(ya), bf_hi(xr[k]) + bf_hi(ya));
      }
      *lp<v4u>(xo) = r;
    }
    STAMP(12);
  }
  WAITL();
  __builtin_amdgcn_s_barrier();
  asm volatile("" ::: "memory");
  STAMP(13);
  asm volatile("Lcode_end:" ::"v"(cpf));
  if (tid < 512) {
    const int t = tid >> 3, dq = tid & 7;
    f32x4 s0 = {0.f, 0.f, 0.f, 0.f}, s1 = s0;
#pragma unroll
    for (int w = 0; w < 4; ++w) {
      s0 += *lp<f32x4>(L_RING + w * 16384 + t * 256 + (((2 * dq) ^ (t & 7)) << 4));
      s1 += *lp<f32x4>(L_RING + w * 16384 + t * 256 + (((2 * dq + 1) ^ (t & 7)) << 4));
    }
    const f32x4 c0 = *lp<f32x4>(L_B2 + dq * 32), c1 = *lp<f32x4>(L_B2 + dq * 32 + 16);
    const v4u r = *lp<v4u>(L_KG + (t >> 3) * 4096 + (t & 7) * 144 + dq * 16);
    v4u o;
#pragma unroll
    for (int k = 0; k < 4; ++k) {
      const float a = k < 2 ? s0[2 * k] : s1[2 * k - 4], c = k < 2 ? s0[2 * k + 1] : s1[2 * k - 3];
      const float ba = k < 2 ? c0[2 * k] : c1[2 * k - 4], bc = k < 2 ? c0[2 * k + 1] : c1[2 * k - 3];
      const u32 p1 = pk_bf(a, c);
      const u32 p2 = pk_bf(bf_lo(p1) + ba, bf_hi(p1) + bc);
      o[k] = pk_bf(bf_lo(r[k]) + bf_lo(p2), bf_hi(r[k]) + bf_hi(p2));
    }
    *(v4u*)(out + (ti * 64 + t) * SD + tj * 64 + dq * 8) = o;
  }
}

#if defined(__HIP_DEVICE_COMPILE__)
#pragma clang attribute pop
#endif

extern "C" void kernel_launch(void* const* d_in, const int* in_sizes, int n_in, void* d_out, int out_size, void* d_ws, size_t ws_size,
                              hipStream_t stream) {
  fused_kernel<<<NWG, NTHR, 0, stream>>>((const u16*)d_in[0], (const u16*)d_in[1], (const u16*)d_in[2], (const u16*)d_in[3], (const u16*)d_in[4],
                                         (const u16*)d_in[5], (const u16*)d_in[6], (const u16*)d_in[7], (const u16*)d_in[8], (const u16*)d_in[10],
                                         (const u16*)d_in[11], (const u16*)d_in[12], (const u16*)d_in[13], (u16*)d_out, (char*)d_ws);
}
```

```cpp
#include <hip/hip_runtime.h>
#include <stdint.h>
#include <type_traits>

typedef unsigned short u16;
typedef unsigned u32;
typedef unsigned long long u64;
typedef __attribute__((ext_vector_type(4))) unsigned v4u;
typedef __attribute__((ext_vector_type(2))) unsigned v2u;
typedef __attribute__((ext_vector_type(2))) float f2;
typedef __attribute__((ext_vector_type(4))) float f32x4;
typedef __attribute__((ext_vector_type(16))) float f32x16;
typedef __attribute__((ext_vector_type(2))) __bf16 bf16x2;
typedef __attribute__((ext_vector_type(8))) __bf16 bf16x8;
typedef __attribute__((ext_vector_type(2))) _Float16 f16x2;
typedef __attribute__((ext_vector_type(8))) _Float16 f16x8;
typedef __attribute__((ext_vector_type(4))) short v4i16;
#define LAS __attribute__((address_space(3)))
#define GAS __attribute__((address_space(1)))
#define DI __device__ __forceinline__
#define RLX_AGENT __ATOMIC_RELAXED, __HIP_MEMORY_SCOPE_AGENT
#define RLX_WG __ATOMIC_RELAXED, __HIP_MEMORY_SCOPE_WORKGROUP

#if defined(__HIP_DEVICE_COMPILE__)
#pragma clang attribute push(__attribute__((target("no-packed-fp32-ops,no-fma-mix-insts,no-mad-mix-insts"))), apply_to = function)
#endif
namespace {
constexpr int SL = 2048, SD = 448, SN = 256, SF = 1792;
constexpr int NWG = 224, NSV = 32, NTHR = 768;
constexpr u32 FLS = 128;
constexpr u32 WS_ST = 4096, WS_CS = 72u << 10, WS_F2 = 15104u << 10, WS_XNT = 256u << 10, WS_YC = 4096u << 10, WS_HF = 7936u << 10, WS_BYTES = 15360u << 10;
constexpr u32 L_XT = 0;
constexpr u32 L_UC = 0, UCS = 4160;
constexpr u32 L_KR = 33280, KRS = 4608;
constexpr u32 L_YT = 42496;
constexpr u32 L_RING = 57344;
constexpr u32 L_KG = 122880;
constexpr u32 L_LT = 155648;
constexpr u32 L_B1 = L_LT, L_CS = L_LT + 1024, L_B2 = L_LT + 2048, L_KT = L_LT + 2304, KTS = 1536;
constexpr u32 L_TAG = 163328, L_SYNC = 163344, L_KM = 163424, L_BYTES = 163488;
constexpr u32 L_ST = 65536;
enum { S_XRDY = 0, S_AB = 1, S_G1 = 2, S_F1 = 3, S_KP = 4, S_CV = 6, S_KQ = 8, S_HP = 10, S_LT = 14, S_LR = 15, S_CD = 16 };
constexpr u32 SPIN_CAP = 1u << 14;
constexpr int SLP_U = 12, LSL = 6, SVC_INF = 2, SIBROT = 2;

template <typename T> DI LAS T* lp(u32 a) { return (LAS T*)(uintptr_t)a; }
DI float bf_lo(u32 v) { return __builtin_bit_cast(float, v << 16); }
DI float bf_hi(u32 v) { return __builtin_bit_cast(float, v & 0xffff0000u); }
DI u32 pk_bf(float a, float b) { bf16x2 r = {(__bf16)a, (__bf16)b}; return __builtin_bit_cast(u32, r); }
DI u32 pk_h(float a, float b) { f16x2 r = {(_Float16)a, (_Float16)b}; return __builtin_bit_cast(u32, r); }
DI float rbf(float a) { return bf_lo(pk_bf(a, a)); }
DI float bf1(u16 v) { return __builtin_bit_cast(float, (u32)v << 16); }
DI f32x16 mfma32(v4u a, v4u b, f32x16 c) { return __builtin_amdgcn_mfma_f32_32x32x16_bf16(__builtin_bit_cast(bf16x8, a), __builtin_bit_cast(bf16x8, b), c, 0, 0, 0); }
DI f32x4 mfma_h(v4u a, v4u b, f32x4 c) { return __builtin_amdgcn_mfma_f32_16x16x32_f16(__builtin_bit_cast(f16x8, a), __builtin_bit_cast(f16x8, b), c, 0, 0, 0); }
DI v2u trd(u32 a) { return __builtin_bit_cast(v2u, __builtin_amdgcn_ds_read_tr16_b64_v4i16(lp<v4i16>(a))); }
DI v4u trd2(u32 a, u32 b) { v2u x = trd(a), y = trd(b); return v4u{x.x, x.y, y.x, y.y}; }
template <int C> DI float dpp(float v) { return __builtin_bit_cast(float, __builtin_amdgcn_update_dpp(0, __builtin_bit_cast(int, v), C, 0xf, 0xf, true)); }
DI float wmax_nn(float v) {
  v = __builtin_fmaxf(v, dpp<0xB1>(v)); v = __builtin_fmaxf(v, dpp<0x4E>(v)); v = __builtin_fmaxf(v, dpp<0x141>(v)); v = __builtin_fmaxf(v, dpp<0x140>(v));
  const int i = __builtin_bit_cast(int, v);
  const u32 a = __builtin_amdgcn_readlane(i, 0), b = __builtin_amdgcn_readlane(i, 16), c = __builtin_amdgcn_readlane(i, 32), d = __builtin_amdgcn_readlane(i, 48);
  const u32 e = a > b ? a : b, f = c > d ? c : d;
  return __builtin_bit_cast(float, e > f ? e : f);
}
DI bool wall(bool p) { return __builtin_amdgcn_ballot_w64(!p) == 0ull; }
DI bool wany(bool p) { return __builtin_amdgcn_ballot_w64(p) != 0ull; }
DI float rdl(float v, int n) { return __builtin_bit_cast(float, __builtin_amdgcn_readlane(__builtin_bit_cast(int, v), n)); }
#define WAITL() asm volatile("s_waitcnt lgkmcnt(0)" ::: "memory")
#define SB() __builtin_amdgcn_sched_barrier(0)
template <int N> DI void waitv() { asm volatile("s_waitcnt vmcnt(%0)" ::"i"(N) : "memory"); }
template <int N> DI void waitv4(v4u (&h)[4]) { asm volatile("s_waitcnt vmcnt(%4)" : "+v"(h[0]), "+v"(h[1]), "+v"(h[2]), "+v"(h[3]) : "i"(N) : "memory"); }
template <int I, int N, class F> DI void sfor(F f) { if constexpr (I < N) { f(std::integral_constant<int, I>{}); sfor<I + 1, N>(f); } }
template <int IL, int IS> DI void glds(u32 lb, u32 voff, __amdgpu_buffer_rsrc_t rs, u32 sb) {
  u32 t;
  asm volatile("s_add_u32 m0, %1, %2\n\ts_add_u32 %0, %3, %4\n\tbuffer_load_dwordx4 %5, %6, %0 offen lds"
               : "=&s"(t) : "s"(lb), "i"(IL), "s"(sb), "i"(IS), "v"(voff), "s"(rs) : "memory", "scc");
}
template <int IO> DI void ld16(v4u& d, u32 voff, __amdgpu_buffer_rsrc_t rs, u32 soff) {
  asm volatile("buffer_load_dwordx4 %0, %1, %2, %3 offen offset:%4" : "=v"(d) : "v"(voff), "s"(rs), "s"(soff), "i"(IO) : "memory");
}
template <int IO> DI void ld16_sc1(v4u& d, u32 voff, __amdgpu_buffer_rsrc_t rs, u32 soff) {
  asm volatile("buffer_load_dwordx4 %0, %1, %2, %3 offen offset:%4 sc1" : "=v"(d) : "v"(voff), "s"(rs), "s"(soff), "i"(IO) : "memory");
}
DI void ld8_sc1(v2u& d, u32 voff, __amdgpu_buffer_rsrc_t rs, u32 soff) {
  asm volatile("buffer_load_dwordx2 %0, %1, %2, %3 offen sc1" : "=v"(d) : "v"(voff), "s"(rs), "s"(soff) : "memory");
}
DI void st16_sc1(v4u d, u32 voff, __amdgpu_buffer_rsrc_t rs, u32 soff) {
  asm volatile("buffer_store_dwordx4 %0, %1, %2, %3 offen sc1\n\ts_nop 1" ::"v"(d), "v"(voff), "s"(rs), "s"(soff) : "memory");
}
DI void lds_arrive(u32 w, int lane) { WAITL(); if (lane == 0) __hip_atomic_fetch_add(lp<u32>(L_SYNC + 4 * w), 1u, RLX_WG); }
template <int SL = 1> DI void lds_wait(u32 w, u32 n) {
#pragma clang loop unroll(disable)
  for (u32 s = 0; s < (1u << 20); ++s) {
    if ((u32)__builtin_amdgcn_readfirstlane(__hip_atomic_load(lp<u32>(L_SYNC + 4 * w), RLX_WG)) >= n) break;
    __builtin_amdgcn_s_sleep(SL);
  }
  asm volatile("" ::: "memory");
}
DI void warm8k(__amdgpu_buffer_rsrc_t rs, int piece, int lane) {
  asm volatile("s_mov_b32 m0, %0\n\ts_nop 0\n\tbuffer_load_dword %1, %2, %3 offen lds\n\tbuffer_load_dword %1, %2, %3 offen offset:64 lds"
               ::"i"(L_ST), "v"(lane * 128), "s"(rs), "s"(piece * 8192) : "memory");
}
DI v4u ldg(__amdgpu_buffer_rsrc_t rs, u32 off) { return __builtin_amdgcn_raw_buffer_load_b128(rs, off, 0, 16); }
DI void stg(u32 a, u32 b, v2u tag, __amdgpu_buffer_rsrc_t rs, u32 off) { __builtin_amdgcn_raw_buffer_store_b128(v4u{a, b, tag.x, tag.y}, rs, off, 0, 16); }
DI bool gok(v4u v, v2u tag) { return v.z == tag.x && v.w == tag.y; }
typedef f2 cf;
struct cw { f2 w, r; };
DI cw mkw(f2 w) { return cw{w, f2{-w.y, w.x}}; }
DI cf cmul(cf a, cw b) { return __builtin_elementwise_fma(a.xx, b.w, a.yy * b.r); }
DI cf cexp_k(float ar2, float rev, float k) {
  const float e = __builtin_amdgcn_exp2f(ar2 * k), f = __builtin_amdgcn_fractf(rev * k);
  return cf{e * __builtin_amdgcn_cosf(f), e * __builtin_amdgcn_sinf(f)};
}
DI int kpos(int tl, int r) {
  return tl == 0 ? (r & 7) + 128 * (r >> 3) : tl == 1 ? ((r & 4) ? 512 + 128 * (r & 3) + 4 * (r >> 3) : 256 + 128 * (r >> 3) + 2 * (r & 3)) : ((r & 4) ? 2048 : 1024 + 512 * (r >> 3) + 128 * (r & 3));
}

DI void kgen_fast(f32x4 (&acc)[3], u32 buf, int lane, cf z1f, cf z8, cf zcf, cf cb) {
  const u32 wa = buf + lane * 4;
  const u32 rb = buf + (lane & 15) * 256 + (((lane >> 4) ^ ((lane & 7) << 1)) << 4);
  auto put2 = [&](int row, cf v, cf w) {
    asm volatile("ds_write2st64_b32 %0, %1, %2 offset0:%3 offset1:%4" ::"v"(wa ^ (row << 5)), "v"(pk_h(v.x, v.y)), "v"(pk_h(w.x, w.y)), "i"(row), "i"(row + 8) : "memory");
  };
  auto get = [&](int s) { return *lp<v4u>(rb ^ (s << 6)); };
  const cw z1 = mkw(z1f), zc = mkw(zcf);
  {
    const cw w = mkw(cf{z8.x, -z8.y});
    const cf w2 = cmul(w.w, w), w4 = cmul(w2, mkw(w2)), w8 = cmul(w4, mkw(w4));
    cf e{16384.f, 0.f}, o = w8 * 16384.f;
#pragma unroll
    for (int c = 0; c < 8; ++c) { put2(c, e, o); if (c < 7) { e = cmul(e, w); o = cmul(o, w); } }
  }
  v4u ff[4];
#pragma unroll
  for (int s = 0; s < 4; ++s) ff[s] = get(s);
  cf g[8];
  g[0] = cb;
#pragma unroll
  for (int e = 1; e < 8; ++e) g[e] = cmul(g[e - 1], z1);
#pragma unroll
  for (int e = 0; e < 8; ++e) put2(e, g[e], cmul(g[e], zc));
#pragma unroll
  for (int s = 0; s < 4; ++s) acc[0] = mfma_h(get(s), ff[s], acc[0]);
  const cw z256 = mkw(cmul(zc.w, zc)), z512 = mkw(cmul(z256.w, z256));
#pragma unroll
  for (int e = 0; e < 4; ++e) { const cf t = cmul(g[2 * e], z256); put2(e, t, cmul(t, zc)); }
  cf a = cmul(g[0], z512), b = cmul(g[4], z512);
  cf c = cmul(a, z512), d = cmul(c, z512);
  const u32 last = pk_h(cmul(d, z512).x, 0.f);
#pragma unroll
  for (int k = 0; k < 4; ++k) { put2(4 + k, a, b); if (k < 3) { a = cmul(a, zc); b = cmul(b, zc); } }
#pragma unroll
  for (int s = 0; s < 4; ++s) acc[1] = mfma_h(get(s), ff[s], acc[1]);
#pragma unroll
  for (int k = 0; k < 4; ++k) { put2(k, c, d); if (k < 3) { c = cmul(c, zc); d = cmul(d, zc); } }
  *lp<u32>((wa ^ (4 << 5)) + 4 * 256) = last;
#pragma unroll
  for (int s = 0; s < 4; ++s) acc[2] = mfma_h(get(s), ff[s], acc[2]);
}
DI void kgen_slow(f32x4 (&acc)[3], int lane, float ar2, float rev, cf cb) {
  const int c = lane & 15, g = lane >> 4;
  const float cbx = cb.x, cby = cb.y;
#pragma clang loop unroll(disable)
  for (int n = 0; n < 64 * 3; ++n) {
    const int tl = n >> 6;
    const float a2 = rdl(ar2, n & 63), rv = rdl(rev, n & 63), cr = rdl(cbx, n & 63), ci = rdl(cby, n & 63);
    f32x4 t;
#pragma unroll
    for (int i = 0; i < 4; ++i) {
      const cf z = cexp_k(a2, rv, (float)(kpos(tl, 4 * g + i) + (tl == 2 && (g & 1) ? 0 : 8 * c)));
      t[i] = cr * z.x - ci * z.y;
    }
    if (tl == 0) acc[0] += t; else if (tl == 1) acc[1] += t; else acc[2] += t;
  }
}
DI float hsum(float v) {
  v += dpp<0xB1>(v); v += dpp<0x4E>(v); v += dpp<0x141>(v); v += dpp<0x140>(v);
  const u32 u = __builtin_bit_cast(u32, v);
  auto r = __builtin_amdgcn_permlane16_swap(u, u, false, false);
  const u32 r0 = r[0], r1 = r[1];
  return __builtin_bit_cast(float, r0) + __builtin_bit_cast(float, r1);
}
template <class H> DI f32x4 ln_rows(v4u (&xv)[2], bool act, H hook) {
  float xf[16], s0 = 0.f, s1 = 0.f;
#pragma unroll
  for (int k = 0; k < 8; ++k) { xf[2 * k] = bf_lo(xv[k >> 2][k & 3]); xf[2 * k + 1] = bf_hi(xv[k >> 2][k & 3]); s0 += xf[2 * k]; s1 += xf[2 * k + 1]; }
  hook(std::integral_constant<int, 0>{});
  const float mean = hsum(s0 + s1) * (1.f / SD), m1 = rbf(mean), ms = act ? m1 : 0.f;
  hook(std::integral_constant<int, 1>{});
  float q0 = 0.f, q1 = 0.f;
#pragma unroll
  for (int k = 0; k < 8; ++k) {
    const float d0 = xf[2 * k] - ms, d1 = xf[2 * k + 1] - ms;
    q0 = __builtin_fmaf(d0, d0, q0); q1 = __builtin_fmaf(d1, d1, q1);
    u32 d = pk_bf(d0, d1);
    asm("" : "+v"(d));
    xv[k >> 2][k & 3] = d;
  }
  hook(std::integral_constant<int, 2>{});
  const float dl = mean - m1;
  const float var = __builtin_fmaxf(hsum(q0 + q1) * (1.f / SD) - dl * dl, 0.f);
  const float r1 = __builtin_amdgcn_rcpf(rbf(__builtin_amdgcn_sqrtf(rbf(rbf(var) + 1e-5f))));
  hook(std::integral_constant<int, 3>{});
  float t1 = 0.f, t2 = 0.f;
#pragma unroll
  for (int k = 0; k < 8; ++k) {
    u32 v = pk_bf(bf_lo(xv[k >> 2][k & 3]) * r1, bf_hi(xv[k >> 2][k & 3]) * r1);
    asm("" : "+v"(v));
    const float a0 = bf_lo(v), a1 = bf_hi(v);
    t1 += a0; t2 = __builtin_fmaf(a0, a0, t2); t1 += a1; t2 = __builtin_fmaf(a1, a1, t2);
    xv[k >> 2][k & 3] = pk_h(a0, a1);
  }
  const float mean2 = hsum(t1) * (1.f / SD), m2 = rbf(mean2);
  const float var2 = __builtin_fmaxf(hsum(t2) * (1.f / SD) - mean2 * mean2, 0.f);
  const float r2 = __builtin_amdgcn_rcpf(rbf(__builtin_amdgcn_sqrtf(rbf(rbf(var2) + 1e-5f))));
  return f32x4{r1 * r2, -(m1 * r1 + m2) * r2, m1, r1};
}
DI void conv_seg(f32x4& a0, f32x4& a1, u32 ua, u32 ka, int T, int hi, int lo) {
  v4u kp[8], kc[8], uf[8];
  auto blk = [&](v4u (&kold)[8], v4u (&knew)[8], int sg) {
    u32 u0 = ua + 512 * sg, k0 = ka - 512 * (T + 1 - sg);
    asm("" : "+v"(u0), "+v"(k0));
#pragma unroll
    for (int s = 0; s < 8; ++s) uf[s] = trd2(u0 + 64 * s, u0 + 64 * s + 8);
#pragma unroll
    for (int s = 0; s < 8; ++s) knew[s] = *lp<v4u>(k0 + 64 * s);
    SB();
    if (sg <= T) {
#pragma unroll
      for (int s = 0; s < 8; ++s) a0 = mfma_h(uf[s], kold[s], a0);
    }
#pragma unroll
    for (int s = 0; s < 8; ++s) a1 = mfma_h(uf[s], knew[s], a1);
  };
#pragma unroll
  for (int s = 0; s < 8; ++s) kp[s] = *lp<v4u>(ka - 512 * (T - (hi <= T ? hi : T)) + 64 * s);
  for (int sg = hi;; sg -= 2) {
    blk(kp, kc, sg);
    if (sg - 1 < lo) break;
    blk(kc, kp, sg - 1);
    if (sg - 2 < lo) break;
  }
}
}

__global__ void __launch_bounds__(NTHR, 3)
fused_kernel(const u16* __restrict__ x, const u16* __restrict__ Are, const u16* __restrict__ Aim, const u16* __restrict__ Bre,
             const u16* __restrict__ Bim, const u16* __restrict__ Cre, const u16* __restrict__ Cim, const u16* __restrict__ logdt,
             const u16* __restrict__ Dsk, const u16* __restrict__ w1, const u16* __restrict__ b1, const u16* __restrict__ w2,
             const u16* __restrict__ b2, u16* __restrict__ out, char* __restrict__ ws) {
  __shared__ __attribute__((aligned(16))) char smem[L_BYTES];
  const int tid = threadIdx.x, lane = tid & 63, wave = __builtin_amdgcn_readfirstlane(tid >> 6);
  const int b = (int)blockIdx.x - NSV, ti = (b & 7) * 4 + (b >> 3) / 7, tj = (b >> 3) % 7, wg = ti * 7 + tj;
  const int l15 = lane & 15, g = lane >> 4;
  if ((u32)(uintptr_t)(LAS char*)smem != 0u) return;
  if (tid < 20) *lp<u32>(L_SYNC + 4 * tid) = 0u;
  WAITL();
  __builtin_amdgcn_s_barrier();
  asm volatile("" ::"s"(x), "s"(Are), "s"(Aim), "s"(Bre), "s"(Bim), "s"(Cre), "s"(Cim), "s"(logdt), "s"(Dsk), "s"(w1), "s"(b1), "s"(w2), "s"(b2), "s"(out), "s"(ws));
  const __amdgpu_buffer_rsrc_t rws = __builtin_amdgcn_make_buffer_rsrc(ws, 0, WS_BYTES, 0x00020000);
  const __amdgpu_buffer_rsrc_t r2 = __builtin_amdgcn_make_buffer_rsrc((void*)w2, 0, SF * SD * 2, 0x00020000);
  const __amdgpu_buffer_rsrc_t rx = __builtin_amdgcn_make_buffer_rsrc((void*)x, 0, SL * SD * 2, 0x00020000);
  const __amdgpu_buffer_rsrc_t r1 = __builtin_amdgcn_make_buffer_rsrc((void*)w1, 0, SD * SF * 2, 0x00020000);

  const int l31 = lane & 31;
  const u32 hh = lane >> 5;
  const bool act = l31 < 28;
  const u32 lnvo = act ? hh * 896 + l31 * 16 : 0x7ffffff0u;
  const u32 xvs = ((g >> 1) * 8 + (l15 >> 1)) * 16384 + (l15 & 1) * 32 + (g & 1) * 16;
  if (b < 0) {
    const int sb = blockIdx.x, st = (sb & 7) * 4 + (sb >> 3);
    v2u tag;
    v4u xl[2], wc[7];
    ld8_sc1(tag, 0, rws, 0);
    ld16<0>(xl[0], lnvo, rx, (st * 64 + 56 + 2 * (wave & 3)) * 896); ld16<448>(xl[1], lnvo, rx, (st * 64 + 56 + 2 * (wave & 3)) * 896);
#pragma unroll
    for (int e = 0; e < 7; ++e)
      ld16<0>(wc[e], (sb < 28 && wave >= 4) ? (lane >> 3) * (SF * 2) + (lane & 7) * 16 : 0x7ffffff0u, r1, (((wave + 4) & 7) * 7 + e) * 8 * SF * 2 + sb * 128);
    asm volatile("s_waitcnt vmcnt(7)" : "+v"(tag), "+v"(xl[0]), "+v"(xl[1])::"memory");
    tag = __builtin_bit_cast(v2u, __builtin_bit_cast(u64, tag) + 1ull);
    v4u su = {};
    if (wave < 4) {
      su = __builtin_bit_cast(v4u, ln_rows(xl, act, [](auto) {}));
      if (act) { const u32 lt = (2 * wave + hh) * 960 + l31 * 16; *lp<v4u>(lt) = xl[0]; *lp<v4u>(lt + 448) = xl[1]; }
    }
    asm volatile("s_waitcnt vmcnt(0)" : "+v"(wc[0]), "+v"(wc[1]), "+v"(wc[2]), "+v"(wc[3]), "+v"(wc[4]), "+v"(wc[5]), "+v"(wc[6])::"memory");
    if (wave >= 4) {
      f32x4 c4[2];
#pragma unroll
      for (int k = 0; k < 8; ++k) {
        float c = 0.f;
#pragma unroll
        for (int e = 0; e < 7; ++e) c += (k & 1) ? bf_hi(wc[e][k >> 1]) : bf_lo(wc[e][k >> 1]);
        c += dpp<0x128>(c);
        const u32 u = __builtin_bit_cast(u32, c);
        auto p = __builtin_amdgcn_permlane16_swap(u, u, false, false);
        const u32 p0 = p[0], p1 = p[1];
        c4[k >> 2][k & 3] = __builtin_bit_cast(float, p0) + __builtin_bit_cast(float, p1);
      }
      if (l31 < 8) { *lp<f32x4>(16384 + ((wave - 4) * 2 + hh) * 256 + l31 * 32) = c4[0]; *lp<f32x4>(16384 + ((wave - 4) * 2 + hh) * 256 + l31 * 32 + 16) = c4[1]; }
    }
    WAITL();
    __builtin_amdgcn_s_barrier();
    if (wave < 4 && l31 < 2) st16_sc1(v4u{l31 ? su.z : su.x, l31 ? su.w : su.y, tag.x, tag.y}, hh * 32 + l31 * 16, rws, WS_ST + (st * 64 + 56 + 2 * wave) * 32);
    if (wave == 11) {
      f2 c = {0.f, 0.f};
#pragma unroll
      for (int e = 0; e < 16; ++e) c += *lp<f2>(16384 + e * 256 + l31 * 8);
      const v2u cu = __builtin_bit_cast(v2u, c);
      if (sb < 28 && lane < 32) st16_sc1(v4u{cu.x, cu.y, tag.x, tag.y}, lane * 16, rws, WS_CS + sb * 512);
    } else if (wave >= 4) {
      const u32 a = ((g & 1) * 4 + (l15 >> 2)) * 960 + (wave - 4) * 128 + (g >> 1) * 32 + (lane & 3) * 8;
      const v2u t0 = trd(a), t1 = trd(a + 64);
      const u32 so = WS_XNT + (st * 8 + 7) * 64 + (wave - 4) * (32 * 16384);
      st16_sc1(v4u{t0.x, t0.y, tag.x, tag.y}, xvs, rws, so); st16_sc1(v4u{t1.x, t1.y, tag.x, tag.y}, xvs, rws, so + 16 * 16384);
    }
    waitv<0>();
    __builtin_amdgcn_s_barrier();
#pragma clang loop unroll(disable)
    for (int j = wave; j < 49; j += 12) { warm8k(r2, j * 4 + (sb >> 3), lane); waitv<SVC_INF>(); }
    if (sb == 0 && wave == 0) {
#pragma unroll 1
      for (int k = 0; k < 4; ++k) {
        const u32 fo = k < 3 || lane < 32 ? WS_F2 + (k * 64 + lane) * 8 * FLS : WS_ST + (lane - 32) * 2048 + 56 * 32 + 8;
#pragma clang loop unroll(disable)
        for (u32 sp = 0; sp < SPIN_CAP; ++sp) {
          const v2u f = __builtin_amdgcn_raw_buffer_load_b64(rws, fo, 0, 16);
          if (wall(f.x == tag.x && f.y == tag.y)) break;
          asm volatile("s_sleep 16" ::: "memory");
        }
      }
      if (lane == 0) __hip_atomic_store((GAS u64*)ws, __builtin_bit_cast(u64, tag), RLX_AGENT);
    }
    waitv<0>();
    return;
  }
  if (wave < 4) {
    const int q = wave;
    const u32 ring = L_RING + q * 16384;
    v4u xl[2];
    v2u tag;
    ld16<0>(xl[0], lnvo, rx, (ti * 64 + tj * 8 + 2 * q) * 896); ld16<448>(xl[1], lnvo, rx, (ti * 64 + tj * 8 + 2 * q) * 896);
    ld8_sc1(tag, 0, rws, 0);
    sfor<0, 7>([&](auto E) {
      constexpr int e = E;
      const u32 n = e * 64 + lane, r = (n * 1171u) >> 16, c = n - r * 56;
      const u32 vo = r * 896 + ((c & ~7u) << 4) + (((c & 7) ^ (r >> 1)) << 4);
      glds<L_XT + e * 1024, 0>(q * 14336, vo, rx, (ti * 64 + q * 16) * 896);
      glds<L_XT + 7168 + e * 1024, 7168>(q * 14336, vo ^ 64, rx, (ti * 64 + q * 16) * 896);
    });
    const u32 cs16 = ((lane & 7) ^ ((lane >> 2) & 4)) << 4;
    const u32 vo1 = (lane >> 3) * (SF * 2) + cs16, vo2 = (lane >> 3) * (SD * 2) + cs16;
    const u32 so1 = (tj * 256 + q * 64) * 2;
    const int rot = ((ti & 3) * 4 + (b & 7) * 2) % 14;
    u32 sibs = tj;
#pragma unroll
    for (int k = 1; k < 7; ++k) sibs |= ((tj + 1 + (k - 1 + SIBROT * (ti & 3)) % 6) % 7) << (4 * k);
    auto sib = [&](int k) { return (sibs >> (4 * k)) & 15u; };
    u32 wb = so1 + rot * (32 * SF * 2);
    auto next_slot = [&](auto N) {
      constexpr int n = N;
      if constexpr (n < 14) { wb += 32 * SF * 2; wb = wb == so1 + 14 * (32 * SF * 2) ? so1 : wb; }
      else wb = sib((n - 14) % 7) * (256 * SD * 2) + (q * 64 + (n >= 21 ? 32 : 0)) * (SD * 2) + tj * 128;
    };
    auto piece = [&](auto N, auto E) {
      constexpr int n = N, e = E;
      if constexpr (e == 0 && n > 0) next_slot(N);
      if constexpr (n < 14) glds<(n & 3) * 4096 + e * 1024, 8 * e * SF * 2>(ring, vo1, r1, wb);
      else glds<(n & 3) * 4096 + e * 1024, 8 * e * SD * 2>(ring, vo2, r2, wb);
    };
    const u32 q4 = l15 >> 2;
    const u32 tc = (g & 1) * 32 + (lane & 3) * 8 + ((q4 >> 1) << 6);
    u32 ta[4] = {ring + (8 * hh + q4) * 128 + tc, ring + (8 * hh + q4) * 128 + (tc ^ 64), ring + (4 * hh + q4) * 128 + tc, ring + (4 * hh + q4) * 128 + (tc ^ 64)};
    asm("" : "+v"(ta[0]), "+v"(ta[1]), "+v"(ta[2]), "+v"(ta[3]));
    u32 xa[4];
#pragma unroll
    for (int m = 0; m < 4; ++m) xa[m] = (lane & 31) * 896 + (((m * 2 + hh) ^ ((lane >> 1) & 7)) << 4);
    u32 xo = (rot >> 1) * 128;
    auto lda = [&](auto U, int c) {
      constexpr int u = U, so = ((u >> 1) & 3) * 4096 + (u & 1) * 2048;
      return u < 28 ? trd2(ta[c] + so, ta[c] + so + 512) : trd2(ta[2 + c] + so, ta[2 + c] + so + 1024);
    };
    f32x16 acc[2][2];
#pragma unroll
    for (int c = 0; c < 2; ++c)
#pragma unroll
      for (int t = 0; t < 2; ++t)
#pragma unroll
        for (int r = 0; r < 16; ++r) acc[c][t][r] = 0.f;
    v4u af[2][2], xf[2][2];
    v4u ab0, ab1, csv;
    auto ldst = [&]() {
      ld16_sc1<0>(ab0, WS_ST + ti * 2048 + l31 * 32, rws, 0); ld16_sc1<1024>(ab1, WS_ST + ti * 2048 + l31 * 32, rws, 0);
      ld16_sc1<0>(csv, WS_CS + (tj * 128 + q * 32 + l31) * 16, rws, 0);
    };
    u32 xadr;
    const u32 lt = L_LT + (2 * q + hh) * 960 + l31 * 16;
    v4u hq[4][4];
    float al[2], be[2];
    auto gelu = [&](auto C, auto K0, auto K1) {
      constexpr int c = C;
#pragma unroll
      for (int k = K0; k < K1; ++k) {
        const f32x4 bb = *lp<f32x4>(L_B1 + (64 * q + 32 * c + 8 * k + 4 * hh) * 4), cc = *lp<f32x4>(L_CS + (64 * q + 32 * c + 8 * k + 4 * hh) * 4);
#pragma unroll
        for (int t = 0; t < 2; ++t) {
          float hv[4];
#pragma unroll
          for (int i = 0; i < 4; ++i) {
            const float v = __builtin_fmaf(acc[c][t][4 * k + i], al[t], __builtin_fmaf(be[t], cc[i], bb[i]));
            const float e = __builtin_amdgcn_exp2f(v * __builtin_fmaf(v * v, -0.1029432397f, -2.302208198f));
            hv[i] = v * __builtin_amdgcn_rcpf(1.f + e);
          }
          hq[3 * c][(k >> 1) * 2 + t][(k & 1) * 2] = pk_bf(hv[0], hv[1]);
          hq[3 * c][(k >> 1) * 2 + t][(k & 1) * 2 + 1] = pk_bf(hv[2], hv[3]);
        }
      }
      SB();
    };
    const u32 hbase = WS_HF + (ti * 7 * 4 + q) * 8192, hown = hbase + tj * 32768;
    u32 hb;
    const u32 fo = WS_F2 + ((ti * 7 + (lane < 7 ? lane : 0)) * 4 + q) * 2 * FLS;
    v2u fl;
    f32x16 a2[2][2];
#pragma unroll
    for (int c = 0; c < 2; ++c)
#pragma unroll
      for (int t = 0; t < 2; ++t)
#pragma unroll
        for (int r = 0; r < 16; ++r) a2[c][t][r] = 0.f;
    piece(std::integral_constant<int, 0>{}, std::integral_constant<int, 0>{});
    piece(std::integral_constant<int, 0>{}, std::integral_constant<int, 1>{});
    piece(std::integral_constant<int, 0>{}, std::integral_constant<int, 2>{});
    piece(std::integral_constant<int, 0>{}, std::integral_constant<int, 3>{});
    asm volatile("s_waitcnt vmcnt(18)" : "+v"(xl[0]), "+v"(xl[1]), "+v"(tag)::"memory");
    tag = __builtin_bit_cast(v2u, __builtin_bit_cast(u64, tag) + 1ull);
    if (lane == 0) *lp<v2u>(L_TAG) = tag;
    {
      const v4u su = __builtin_bit_cast(v4u, ln_rows(xl, act, [&](auto K) { piece(std::integral_constant<int, 1 + (K >> 1)>{}, std::integral_constant<int, (K & 1) * 2>{}); piece(std::integral_constant<int, 1 + (K >> 1)>{}, std::integral_constant<int, (K & 1) * 2 + 1>{}); }));
      if (act) { *lp<v4u>(lt) = xl[0]; *lp<v4u>(lt + 448) = xl[1]; }
      lds_arrive(S_LT, lane);
      if (l31 < 2) st16_sc1(v4u{l31 ? su.z : su.x, l31 ? su.w : su.y, tag.x, tag.y}, hh * 32 + l31 * 16, rws, WS_ST + (ti * 64 + tj * 8 + 2 * q) * 32);
    }
    __builtin_amdgcn_s_setprio(3);
    waitv<13>();
    lds_arrive(S_XRDY, lane);
    piece(std::integral_constant<int, 3>{}, std::integral_constant<int, 0>{});
    piece(std::integral_constant<int, 3>{}, std::integral_constant<int, 1>{});
    piece(std::integral_constant<int, 3>{}, std::integral_constant<int, 2>{});
    piece(std::integral_constant<int, 3>{}, std::integral_constant<int, 3>{});
    lds_wait(S_XRDY, 4);
    waitv<13>();
    af[0][0] = lda(std::integral_constant<int, 0>{}, 0); xf[0][0] = *lp<v4u>(xa[0] + xo); xf[0][1] = *lp<v4u>(xa[0] + xo + 28672); af[0][1] = lda(std::integral_constant<int, 0>{}, 1);
    SB(); acc[0][0] = mfma32(af[0][0], xf[0][0], acc[0][0]); SB();
    af[1][0] = lda(std::integral_constant<int, 1>{}, 0);
    xadr = xa[1] + xo; xf[1][0] = *lp<v4u>(xadr);
    SB(); acc[0][1] = mfma32(af[0][0], xf[0][1], acc[0][1]); SB();
    xf[1][1] = *lp<v4u>(xadr + 28672);
    af[1][1] = lda(std::integral_constant<int, 1>{}, 1);
    SB(); acc[1][0] = mfma32(af[0][1], xf[0][0], acc[1][0]); SB();
    SB(); acc[1][1] = mfma32(af[0][1], xf[0][1], acc[1][1]);
    asm volatile("" ::"v"(af[0][0]), "v"(af[0][1]), "v"(xf[0][0]), "v"(xf[0][1])); SB();
    SB(); acc[0][0] = mfma32(af[1][0], xf[1][0], acc[0][0]); SB();
    waitv<9>();
    af[0][0] = lda(std::integral_constant<int, 2>{}, 0);
    xadr = xa[2] + xo; xf[0][0] = *lp<v4u>(xadr);
    SB(); acc[0][1] = mfma32(af[1][0], xf[1][1], acc[0][1]); SB();
    xf[0][1] = *lp<v4u>(xadr + 28672);
    af[0][1] = lda(std::integral_constant<int, 2>{}, 1);
    SB(); acc[1][0] = mfma32(af[1][1], xf[1][0], acc[1][0]); SB();
    piece(std::integral_constant<int, 4>{}, std::integral_constant<int, 0>{});
    SB(); acc[1][1] = mfma32(af[1][1], xf[1][1], acc[1][1]);
    asm volatile("" ::"v"(af[1][0]), "v"(af[1][1]), "v"(xf[1][0]), "v"(xf[1][1])); SB();
    piece(std::integral_constant<int, 4>{}, std::integral_constant<int, 1>{});
    SB(); acc[0][0] = mfma32(af[0][0], xf[0][0], acc[0][0]); SB();
    af[1][0] = lda(std::integral_constant<int, 3>{}, 0);
    xadr = xa[3] + xo; xf[1][0] = *lp<v4u>(xadr);
    SB(); acc[0][1] = mfma32(af[0][0], xf[0][1], acc[0][1]); SB();
    xf[1][1] = *lp<v4u>(xadr + 28672);
    af[1][1] = lda(std::integral_constant<int, 3>{}, 1);
    SB(); acc[1][0] = mfma32(af[0][1], xf[0][0], acc[1][0]); SB();
    piece(std::integral_constant<int, 4>{}, std::integral_constant<int, 2>{});
    SB(); acc[1][1] = mfma32(af[0][1], xf[0][1], acc[1][1]);
    asm volatile("" ::"v"(af[0][0]), "v"(af[0][1]), "v"(xf[0][0]), "v"(xf[0][1])); SB();
    piece(std::integral_constant<int, 4>{}, std::integral_constant<int, 3>{});
    SB(); acc[0][0] = mfma32(af[1][0], xf[1][0], acc[0][0]); SB();
    waitv<9>();
    xo += 128; xo = xo == 896 ? 0 : xo;
    af[0][0] = lda(std::integral_constant<int, 4>{}, 0);
    xadr = xa[0] + xo; xf[0][0] = *lp<v4u>(xadr);
    SB(); acc[0][1] = mfma32(af[1][0], xf[1][1], acc[0][1]); SB();
    xf[0][1] = *lp<v4u>(xadr + 28672);
    af[0][1] = lda(std::integral_constant<int, 4>{}, 1);
    SB(); acc[1][0] = mfma32(af[1][1], xf[1][0], acc[1][0]); SB();
    piece(std::integral_constant<int, 5>{}, std::integral_constant<int, 0>{});
    SB(); acc[1][1] = mfma32(af[1][1], xf[1][1], acc[1][1]);
    asm volatile("" ::"v"(af[1][0]), "v"(af[1][1]), "v"(xf[1][0]), "v"(xf[1][1])); SB();
    piece(std::integral_constant<int, 5>{}, std::integral_constant<int, 1>{});
    SB(); acc[0][0] = mfma32(af[0][0], xf[0][0], acc[0][0]); SB();
    af[1][0] = lda(std::integral_constant<int, 5>{}, 0);
    xadr = xa[1] + xo; xf[1][0] = *lp<v4u>(xadr);
    SB(); acc[0][1] = mfma32(af[0][0], xf[0][1], acc[0][1]); SB();
    xf[1][1] = *lp<v4u>(xadr + 28672);
    af[1][1] = lda(std::integral_constant<int, 5>{}, 1);
    SB(); acc[1][0] = mfma32(af[0][1], xf[0][0], acc[1][0]); SB();
    piece(std::integral_constant<int, 5>{}, std::integral_constant<int, 2>{});
    SB(); acc[1][1] = mfma32(af[0][1], xf[0][1], acc[1][1]);
    asm volatile("" ::"v"(af[0][0]), "v"(af[0][1]), "v"(xf[0][0]), "v"(xf[0][1])); SB();
    piece(std::integral_constant<int, 5>{}, std::integral_constant<int, 3>{});
    SB(); acc[0][0] = mfma32(af[1][0], xf[1][0], acc[0][0]); SB();
    waitv<8>();
    af[0][0] = lda(std::integral_constant<int, 6>{}, 0);
    xadr = xa[2] + xo; xf[0][0] = *lp<v4u>(xadr);
    SB(); acc[0][1] = mfma32(af[1][0], xf[1][1], acc[0][1]); SB();
    xf[0][1] = *lp<v4u>(xadr + 28672);
    af[0][1] = lda(std::integral_constant<int, 6>{}, 1);
    SB(); acc[1][0] = mfma32(af[1][1], xf[1][0], acc[1][0]); SB();
    piece(std::integral_constant<int, 6>{}, std::integral_constant<int, 0>{});
    SB(); acc[1][1] = mfma32(af[1][1], xf[1][1], acc[1][1]);
    asm volatile("" ::"v"(af[1][0]), "v"(af[1][1]), "v"(xf[1][0]), "v"(xf[1][1])); SB();
    piece(std::integral_constant<int, 6>{}, std::integral_constant<int, 1>{});
    SB(); acc[0][0] = mfma32(af[0][0], xf[0][0], acc[0][0]); SB();
    af[1][0] = lda(std::integral_constant<int, 7>{}, 0);
    xadr = xa[3] + xo; xf[1][0] = *lp<v4u>(xadr);
    SB(); acc[0][1] = mfma32(af[0][0], xf[0][1], acc[0][1]); SB();
    xf[1][1] = *lp<v4u>(xadr + 28672);
    af[1][1] = lda(std::integral_constant<int, 7>{}, 1);
    SB(); acc[1][0] = mfma32(af[0][1], xf[0][0], acc[1][0]); SB();
    piece(std::integral_constant<int, 6>{}, std::integral_constant<int, 2>{});
    SB(); acc[1][1] = mfma32(af[0][1], xf[0][1], acc[1][1]);
    asm volatile("" ::"v"(af[0][0]), "v"(af[0][1]), "v"(xf[0][0]), "v"(xf[0][1])); SB();
    piece(std::integral_constant<int, 6>{}, std::integral_constant<int, 3>{});
    SB(); acc[0][0] = mfma32(af[1][0], xf[1][0], acc[0][0]); SB();
    waitv<8>();
    xo += 128; xo = xo == 896 ? 0 : xo;
    af[0][0] = lda(std::integral_constant<int, 8>{}, 0);
    xadr = xa[0] + xo; xf[0][0] = *lp<v4u>(xadr);
    SB(); acc[0][1] = mfma32(af[1][0], xf[1][1], acc[0][1]); SB();
    xf[0][1] = *lp<v4u>(xadr + 28672);
    af[0][1] = lda(std::integral_constant<int, 8>{}, 1);
    SB(); acc[1][0] = mfma32(af[1][1], xf[1][0], acc[1][0]); SB();
    piece(std::integral_constant<int, 7>{}, std::integral_constant<int, 0>{});
    SB(); acc[1][1] = mfma32(af[1][1], xf[1][1], acc[1][1]);
    asm volatile("" ::"v"(af[1][0]), "v"(af[1][1]), "v"(xf[1][0]), "v"(xf[1][1])); SB();
    piece(std::integral_constant<int, 7>{}, std::integral_constant<int, 1>{});
    SB(); acc[0][0] = mfma32(af[0][0], xf[0][0], acc[0][0]); SB();
    af[1][0] = lda(std::integral_constant<int, 9>{}, 0);
    xadr = xa[1] + xo; xf[1][0] = *lp<v4u>(xadr);
    SB(); acc[0][1] = mfma32(af[0][0], xf[0][1], acc[0][1]); SB();
    xf[1][1] = *lp<v4u>(xadr + 28672);
    af[1][1] = lda(std::integral_constant<int, 9>{}, 1);
    SB(); acc[1][0] = mfma32(af[0][1], xf[0][0], acc[1][0]); SB();
    piece(std::integral_constant<int, 7>{}, std::integral_constant<int, 2>{});
    SB(); acc[1][1] = mfma32(af[0][1], xf[0][1], acc[1][1]);
    asm volatile("" ::"v"(af[0][0]), "v"(af[0][1]), "v"(xf[0][0]), "v"(xf[0][1])); SB();
    piece(std::integral_constant<int, 7>{}, std::integral_constant<int, 3>{});
    SB(); acc[0][0] = mfma32(af[1][0], xf[1][0], acc[0][0]); SB();
    waitv<8>();
    af[0][0] = lda(std::integral_constant<int, 10>{}, 0);
    xadr = xa[2] + xo; xf[0][0] = *lp<v4u>(xadr);
    SB(); acc[0][1] = mfma32(af[1][0], xf[1][1], acc[0][1]); SB();
    xf[0][1] = *lp<v4u>(xadr + 28672);
    af[0][1] = lda(std::integral_constant<int, 10>{}, 1);
    SB(); acc[1][0] = mfma32(af[1][1], xf[1][0], acc[1][0]); SB();
    piece(std::integral_constant<int, 8>{}, std::integral_constant<int, 0>{});
    SB(); acc[1][1] = mfma32(af[1][1], xf[1][1], acc[1][1]);
    asm volatile("" ::"v"(af[1][0]), "v"(af[1][1]), "v"(xf[1][0]), "v"(xf[1][1])); SB();
    piece(std::integral_constant<int, 8>{}, std::integral_constant<int, 1>{});
    SB(); acc[0][0] = mfma32(af[0][0], xf[0][0], acc[0][0]); SB();
    af[1][0] = lda(std::integral_constant<int, 11>{}, 0);
    xadr = xa[3] + xo; xf[1][0] = *lp<v4u>(xadr);
    SB(); acc[0][1] = mfma32(af[0][0], xf[0][1], acc[0][1]); SB();
    xf[1][1] = *lp<v4u>(xadr + 28672);
    af[1][1] = lda(std::integral_constant<int, 11>{}, 1);
    SB(); acc[1][0] = mfma32(af[0][1], xf[0][0], acc[1][0]); SB();
    piece(std::integral_constant<int, 8>{}, std::integral_constant<int, 2>{});
    SB(); acc[1][1] = mfma32(af[0][1], xf[0][1], acc[1][1]);
    asm volatile("" ::"v"(af[0][0]), "v"(af[0][1]), "v"(xf[0][0]), "v"(xf[0][1])); SB();
    piece(std::integral_constant<int, 8>{}, std::integral_constant<int, 3>{});
    SB(); acc[0][0] = mfma32(af[1][0], xf[1][0], acc[0][0]); SB();
    waitv<8>();
    xo += 128; xo = xo == 896 ? 0 : xo;
    af[0][0] = lda(std::integral_constant<int, 12>{}, 0);
    xadr = xa[0] + xo; xf[0][0] = *lp<v4u>(xadr);
    SB(); acc[0][1] = mfma32(af[1][0], xf[1][1], acc[0][1]); SB();
    xf[0][1] = *lp<v4u>(xadr + 28672);
    af[0][1] = lda(std::integral_constant<int, 12>{}, 1);
    SB(); acc[1][0] = mfma32(af[1][1], xf[1][0], acc[1][0]); SB();
    piece(std::integral_constant<int, 9>{}, std::integral_constant<int, 0>{});
    SB(); acc[1][1] = mfma32(af[1][1], xf[1][1], acc[1][1]);
    asm volatile("" ::"v"(af[1][0]), "v"(af[1][1]), "v"(xf[1][0]), "v"(xf[1][1])); SB();
    piece(std::integral_constant<int, 9>{}, std::integral_constant<int, 1>{});
    SB(); acc[0][0] = mfma32(af[0][0], xf[0][0], acc[0][0]); SB();
    af[1][0] = lda(std::integral_constant<int, 13>{}, 0);
    xadr = xa[1] + xo; xf[1][0] = *lp<v4u>(xadr);
    SB(); acc[0][1] = mfma32(af[0][0], xf[0][1], acc[0][1]); SB();
    xf[1][1] = *lp<v4u>(xadr + 28672);
    af[1][1] = lda(std::integral_constant<int, 13>{}, 1);
    SB(); acc[1][0] = mfma32(af[0][1], xf[0][0], acc[1][0]); SB();
    piece(std::integral_constant<int, 9>{}, std::integral_constant<int, 2>{});
    SB(); acc[1][1] = mfma32(af[0][1], xf[0][1], acc[1][1]);
    asm volatile("" ::"v"(af[0][0]), "v"(af[0][1]), "v"(xf[0][0]), "v"(xf[0][1])); SB();
    piece(std::integral_constant<int, 9>{}, std::integral_constant<int, 3>{});
    SB(); acc[0][0] = mfma32(af[1][0], xf[1][0], acc[0][0]); SB();
    waitv<8>();
    af[0][0] = lda(std::integral_constant<int, 14>{}, 0);
    xadr = xa[2] + xo; xf[0][0] = *lp<v4u>(xadr);
    SB(); acc[0][1] = mfma32(af[1][0], xf[1][1], acc[0][1]); SB();
    xf[0][1] = *lp<v4u>(xadr + 28672);
    af[0][1] = lda(std::integral_constant<int, 14>{}, 1);
    SB(); acc[1][0] = mfma32(af[1][1], xf[1][0], acc[1][0]); SB();
    piece(std::integral_constant<int, 10>{}, std::integral_constant<int, 0>{});
    SB(); acc[1][1] = mfma32(af[1][1], xf[1][1], acc[1][1]);
    asm volatile("" ::"v"(af[1][0]), "v"(af[1][1]), "v"(xf[1][0]), "v"(xf[1][1])); SB();
    piece(std::integral_constant<int, 10>{}, std::integral_constant<int, 1>{});
    SB(); acc[0][0] = mfma32(af[0][0], xf[0][0], acc[0][0]); SB();
    af[1][0] = lda(std::integral_constant<int, 15>{}, 0);
    xadr = xa[3] + xo; xf[1][0] = *lp<v4u>(xadr);
    SB(); acc[0][1] = mfma32(af[0][0], xf[0][1], acc[0][1]); SB();
    xf[1][1] = *lp<v4u>(xadr + 28672);
    af[1][1] = lda(std::integral_constant<int, 15>{}, 1);
    SB(); acc[1][0] = mfma32(af[0][1], xf[0][0], acc[1][0]); SB();
    piece(std::integral_constant<int, 10>{}, std::integral_constant<int, 2>{});
    SB(); acc[1][1] = mfma32(af[0][1], xf[0][1], acc[1][1]);
    asm volatile("" ::"v"(af[0][0]), "v"(af[0][1]), "v"(xf[0][0]), "v"(xf[0][1])); SB();
    piece(std::integral_constant<int, 10>{}, std::integral_constant<int, 3>{});
    SB(); acc[0][0] = mfma32(af[1][0], xf[1][0], acc[0][0]); SB();
    waitv<8>();
    xo += 128; xo = xo == 896 ? 0 : xo;
    af[0][0] = lda(std::integral_constant<int, 16>{}, 0);
    xadr = xa[0] + xo; xf[0][0] = *lp<v4u>(xadr);
    SB(); acc[0][1] = mfma32(af[1][0], xf[1][1], acc[0][1]); SB();
    xf[0][1] = *lp<v4u>(xadr + 28672);
    af[0][1] = lda(std::integral_constant<int, 16>{}, 1);
    SB(); acc[1][0] = mfma32(af[1][1], xf[1][0], acc[1][0]); SB();
    piece(std::integral_constant<int, 11>{}, std::integral_constant<int, 0>{});
    SB(); acc[1][1] = mfma32(af[1][1], xf[1][1], acc[1][1]);
    asm volatile("" ::"v"(af[1][0]), "v"(af[1][1]), "v"(xf[1][0]), "v"(xf[1][1])); SB();
    piece(std::integral_constant<int, 11>{}, std::integral_constant<int, 1>{});
    SB(); acc[0][0] = mfma32(af[0][0], xf[0][0], acc[0][0]); SB();
    af[1][0] = lda(std::integral_constant<int, 17>{}, 0);
    xadr = xa[1] + xo; xf[1][0] = *lp<v4u>(xadr);
    SB(); acc[0][1] = mfma32(af[0][0], xf[0][1], acc[0][1]); SB();
    xf[1][1] = *lp<v4u>(xadr + 28672);
    af[1][1] = lda(std::integral_constant<int, 17>{}, 1);
    SB(); acc[1][0] = mfma32(af[0][1], xf[0][0], acc[1][0]); SB();
    piece(std::integral_constant<int, 11>{}, std::integral_constant<int, 2>{});
    SB(); acc[1][1] = mfma32(af[0][1], xf[0][1], acc[1][1]);
    asm volatile("" ::"v"(af[0][0]), "v"(af[0][1]), "v"(xf[0][0]), "v"(xf[0][1])); SB();
    piece(std::integral_constant<int, 11>{}, std::integral_constant<int, 3>{});
    SB(); acc[0][0] = mfma32(af[1][0], xf[1][0], acc[0][0]); SB();
    waitv<8>();
    af[0][0] = lda(std::integral_constant<int, 18>{}, 0);
    xadr = xa[2] + xo; xf[0][0] = *lp<v4u>(xadr);
    SB(); acc[0][1] = mfma32(af[1][0], xf[1][1], acc[0][1]); SB();
    xf[0][1] = *lp<v4u>(xadr + 28672);
    af[0][1] = lda(std::integral_constant<int, 18>{}, 1);
    SB(); acc[1][0] = mfma32(af[1][1], xf[1][0], acc[1][0]); SB();
    piece(std::integral_constant<int, 12>{}, std::integral_constant<int, 0>{});
    SB(); acc[1][1] = mfma32(af[1][1], xf[1][1], acc[1][1]);
    asm volatile("" ::"v"(af[1][0]), "v"(af[1][1]), "v"(xf[1][0]), "v"(xf[1][1])); SB();
    piece(std::integral_constant<int, 12>{}, std::integral_constant<int, 1>{});
    SB(); acc[0][0] = mfma32(af[0][0], xf[0][0], acc[0][0]); SB();
    af[1][0] = lda(std::integral_constant<int, 19>{}, 0);
    xadr = xa[3] + xo; xf[1][0] = *lp<v4u>(xadr);
    SB(); acc[0][1] = mfma32(af[0][0], xf[0][1], acc[0][1]); SB();
    xf[1][1] = *lp<v4u>(xadr + 28672);
    af[1][1] = lda(std::integral_constant<int, 19>{}, 1);
    SB(); acc[1][0] = mfma32(af[0][1], xf[0][0], acc[1][0]); SB();
    piece(std::integral_constant<int, 12>{}, std::integral_constant<int, 2>{});
    SB(); acc[1][1] = mfma32(af[0][1], xf[0][1], acc[1][1]);
    asm volatile("" ::"v"(af[0][0]), "v"(af[0][1]), "v"(xf[0][0]), "v"(xf[0][1])); SB();
    piece(std::integral_constant<int, 12>{}, std::integral_constant<int, 3>{});
    SB(); acc[0][0] = mfma32(af[1][0], xf[1][0], acc[0][0]); SB();
    waitv<8>();
    xo += 128; xo = xo == 896 ? 0 : xo;
    af[0][0] = lda(std::integral_constant<int, 20>{}, 0);
    xadr = xa[0] + xo; xf[0][0] = *lp<v4u>(xadr);
    SB(); acc[0][1] = mfma32(af[1][0], xf[1][1], acc[0][1]); SB();
    xf[0][1] = *lp<v4u>(xadr + 28672);
    af[0][1] = lda(std::integral_constant<int, 20>{}, 1);
    SB(); acc[1][0] = mfma32(af[1][1], xf[1][0], acc[1][0]); SB();
    piece(std::integral_constant<int, 13>{}, std::integral_constant<int, 0>{});
    SB(); acc[1][1] = mfma32(af[1][1], xf[1][1], acc[1][1]);
    asm volatile("" ::"v"(af[1][0]), "v"(af[1][1]), "v"(xf[1][0]), "v"(xf[1][1])); SB();
    piece(std::integral_constant<int, 13>{}, std::integral_constant<int, 1>{});
    SB(); acc[0][0] = mfma32(af[0][0], xf[0][0], acc[0][0]); SB();
    af[1][0] = lda(std::integral_constant<int, 21>{}, 0);
    xadr = xa[1] + xo; xf[1][0] = *lp<v4u>(xadr);
    SB(); acc[0][1] = mfma32(af[0][0], xf[0][1], acc[0][1]); SB();
    xf[1][1] = *lp<v4u>(xadr + 28672);
    af[1][1] = lda(std::integral_constant<int, 21>{}, 1);
    SB(); acc[1][0] = mfma32(af[0][1], xf[0][0], acc[1][0]); SB();
    piece(std::integral_constant<int, 13>{}, std::integral_constant<int, 2>{});
    SB(); acc[1][1] = mfma32(af[0][1], xf[0][1], acc[1][1]);
    asm volatile("" ::"v"(af[0][0]), "v"(af[0][1]), "v"(xf[0][0]), "v"(xf[0][1])); SB();
    piece(std::integral_constant<int, 13>{}, std::integral_constant<int, 3>{});
    SB(); acc[0][0] = mfma32(af[1][0], xf[1][0], acc[0][0]); SB();
    waitv<8>();
    af[0][0] = lda(std::integral_constant<int, 22>{}, 0);
    xadr = xa[2] + xo; xf[0][0] = *lp<v4u>(xadr);
    SB(); acc[0][1] = mfma32(af[1][0], xf[1][1], acc[0][1]); SB();
    xf[0][1] = *lp<v4u>(xadr + 28672);
    af[0][1] = lda(std::integral_constant<int, 22>{}, 1);
    SB(); acc[1][0] = mfma32(af[1][1], xf[1][0], acc[1][0]); SB();
    piece(std::integral_constant<int, 14>{}, std::integral_constant<int, 0>{});
    SB(); acc[1][1] = mfma32(af[1][1], xf[1][1], acc[1][1]);
    asm volatile("" ::"v"(af[1][0]), "v"(af[1][1]), "v"(xf[1][0]), "v"(xf[1][1])); SB();
    piece(std::integral_constant<int, 14>{}, std::integral_constant<int, 1>{});
    ldst();
    SB(); acc[0][0] = mfma32(af[0][0], xf[0][0], acc[0][0]); SB();
    af[1][0] = lda(std::integral_constant<int, 23>{}, 0);
    xadr = xa[3] + xo; xf[1][0] = *lp<v4u>(xadr);
    SB(); acc[0][1] = mfma32(af[0][0], xf[0][1], acc[0][1]); SB();
    xf[1][1] = *lp<v4u>(xadr + 28672);
    af[1][1] = lda(std::integral_constant<int, 23>{}, 1);
    SB(); acc[1][0] = mfma32(af[0][1], xf[0][0], acc[1][0]); SB();
    piece(std::integral_constant<int, 14>{}, std::integral_constant<int, 2>{});
    SB(); acc[1][1] = mfma32(af[0][1], xf[0][1], acc[1][1]);
    asm volatile("" ::"v"(af[0][0]), "v"(af[0][1]), "v"(xf[0][0]), "v"(xf[0][1])); SB();
    piece(std::integral_constant<int, 14>{}, std::integral_constant<int, 3>{});
    SB(); acc[0][0] = mfma32(af[1][0], xf[1][0], acc[0][0]); SB();
    waitv<11>();
    xo += 128; xo = xo == 896 ? 0 : xo;
    af[0][0] = lda(std::integral_constant<int, 24>{}, 0);
    xadr = xa[0] + xo; xf[0][0] = *lp<v4u>(xadr);
    SB(); acc[0][1] = mfma32(af[1][0], xf[1][1], acc[0][1]); SB();
    xf[0][1] = *lp<v4u>(xadr + 28672);
    af[0][1] = lda(std::integral_constant<int, 24>{}, 1);
    SB(); acc[1][0] = mfma32(af[1][1], xf[1][0], acc[1][0]); SB();
    piece(std::integral_constant<int, 15>{}, std::integral_constant<int, 0>{});
    SB(); acc[1][1] = mfma32(af[1][1], xf[1][1], acc[1][1]);
    asm volatile("" ::"v"(af[1][0]), "v"(af[1][1]), "v"(xf[1][0]), "v"(xf[1][1])); SB();
    piece(std::integral_constant<int, 15>{}, std::integral_constant<int, 1>{});
    SB(); acc[0][0] = mfma32(af[0][0], xf[0][0], acc[0][0]); SB();
    af[1][0] = lda(std::integral_constant<int, 25>{}, 0);
    xadr = xa[1] + xo; xf[1][0] = *lp<v4u>(xadr);
    SB(); acc[0][1] = mfma32(af[0][0], xf[0][1], acc[0][1]); SB();
    xf[1][1] = *lp<v4u>(xadr + 28672);
    af[1][1] = lda(std::integral_constant<int, 25>{}, 1);
    SB(); acc[1][0] = mfma32(af[0][1], xf[0][0], acc[1][0]); SB();
    piece(std::integral_constant<int, 15>{}, std::integral_constant<int, 2>{});
    SB(); acc[1][1] = mfma32(af[0][1], xf[0][1], acc[1][1]);
    asm volatile("" ::"v"(af[0][0]), "v"(af[0][1]), "v"(xf[0][0]), "v"(xf[0][1])); SB();
    piece(std::integral_constant<int, 15>{}, std::integral_constant<int, 3>{});
    SB(); acc[0][0] = mfma32(af[1][0], xf[1][0], acc[0][0]); SB();
    waitv<11>();
    af[0][0] = lda(std::integral_constant<int, 26>{}, 0);
    xadr = xa[2] + xo; xf[0][0] = *lp<v4u>(xadr);
    SB(); acc[0][1] = mfma32(af[1][0], xf[1][1], acc[0][1]); SB();
    xf[0][1] = *lp<v4u>(xadr + 28672);
    af[0][1] = lda(std::integral_constant<int, 26>{}, 1);
    SB(); acc[1][0] = mfma32(af[1][1], xf[1][0], acc[1][0]); SB();
    piece(std::integral_constant<int, 16>{}, std::integral_constant<int, 0>{});
    SB(); acc[1][1] = mfma32(af[1][1], xf[1][1], acc[1][1]);
    asm volatile("" ::"v"(af[1][0]), "v"(af[1][1]), "v"(xf[1][0]), "v"(xf[1][1])); SB();
    piece(std::integral_constant<int, 16>{}, std::integral_constant<int, 1>{});
    SB(); acc[0][0] = mfma32(af[0][0], xf[0][0], acc[0][0]); SB();
    af[1][0] = lda(std::integral_constant<int, 27>{}, 0);
    xadr = xa[3] + xo; xf[1][0] = *lp<v4u>(xadr);
    SB(); acc[0][1] = mfma32(af[0][0], xf[0][1], acc[0][1]); SB();
    xf[1][1] = *lp<v4u>(xadr + 28672);
    af[1][1] = lda(std::integral_constant<int, 27>{}, 1);
    SB(); acc[1][0] = mfma32(af[0][1], xf[0][0], acc[1][0]); SB();
    piece(std::integral_constant<int, 16>{}, std::integral_constant<int, 2>{});
    SB(); acc[1][1] = mfma32(af[0][1], xf[0][1], acc[1][1]);
    asm volatile("" ::"v"(af[0][0]), "v"(af[0][1]), "v"(xf[0][0]), "v"(xf[0][1])); SB();
    piece(std::integral_constant<int, 16>{}, std::integral_constant<int, 3>{});
    SB(); acc[0][0] = mfma32(af[1][0], xf[1][0], acc[0][0]); SB();
    waitv<8>();
    xo += 128; xo = xo == 896 ? 0 : xo;
    af[0][0] = lda(std::integral_constant<int, 28>{}, 0);
    SB(); acc[0][1] = mfma32(af[1][0], xf[1][1], acc[0][1]); SB();
    af[0][1] = lda(std::integral_constant<int, 28>{}, 1);
    SB(); acc[1][0] = mfma32(af[1][1], xf[1][0], acc[1][0]); SB();
    piece(std::integral_constant<int, 17>{}, std::integral_constant<int, 0>{});
    SB(); acc[1][1] = mfma32(af[1][1], xf[1][1], acc[1][1]);
    asm volatile("" ::"v"(af[1][0]), "v"(af[1][1]), "v"(xf[1][0]), "v"(xf[1][1])); SB();
    piece(std::integral_constant<int, 17>{}, std::integral_constant<int, 1>{});
    piece(std::integral_constant<int, 17>{}, std::integral_constant<int, 2>{});
    piece(std::integral_constant<int, 17>{}, std::integral_constant<int, 3>{});
    lds_arrive(S_G1, lane);
    lds_wait(S_AB, 8);
    asm volatile("s_waitcnt vmcnt(14)" : "+v"(ab0), "+v"(ab1), "+v"(csv)::"memory");
    _Pragma("clang loop unroll(disable)") for (u32 sp = 0; sp < SPIN_CAP; ++sp) {
      if (wall(gok(ab0, tag) && gok(ab1, tag) && gok(csv, tag))) break;
      __builtin_amdgcn_s_sleep(8);
      ldst();
      asm volatile("s_waitcnt vmcnt(0)" : "+v"(ab0), "+v"(ab1), "+v"(csv)::"memory");
    }
    if (lane < 32) *lp<v2u>(L_CS + (q * 32 + lane) * 8) = v2u{csv.x, csv.y};
    const f32x4 f0 = __builtin_bit_cast(f32x4, ab0), f1 = __builtin_bit_cast(f32x4, ab1);
    al[0] = f0.x; al[1] = f1.x; be[0] = f0.y; be[1] = f1.y;
    gelu(std::integral_constant<int, 0>{}, std::integral_constant<int, 0>{}, std::integral_constant<int, 2>{});
    gelu(std::integral_constant<int, 0>{}, std::integral_constant<int, 2>{}, std::integral_constant<int, 4>{});
    sfor<0, 4>([&](auto I) { st16_sc1(hq[0][I], lane * 16 + I * 1024, rws, hown + 0); });
    gelu(std::integral_constant<int, 1>{}, std::integral_constant<int, 0>{}, std::integral_constant<int, 1>{});
    gelu(std::integral_constant<int, 1>{}, std::integral_constant<int, 1>{}, std::integral_constant<int, 2>{});
    waitv<0>();
    if (lane == 0) __hip_atomic_store((GAS u64*)(ws + WS_F2 + ((wg * 4 + q) * 2 + 0) * FLS), __builtin_bit_cast(u64, tag), RLX_AGENT);
    gelu(std::integral_constant<int, 1>{}, std::integral_constant<int, 2>{}, std::integral_constant<int, 3>{});
    gelu(std::integral_constant<int, 1>{}, std::integral_constant<int, 3>{}, std::integral_constant<int, 4>{});
    sfor<0, 4>([&](auto I) { st16_sc1(hq[3][I], lane * 16 + I * 1024, rws, hown + 4096); });
    ld8_sc1(fl, fo + 0, rws, 0);
    SB(); a2[0][0] = mfma32(af[0][0], hq[0][0], a2[0][0]); SB();
    af[1][0] = lda(std::integral_constant<int, 29>{}, 0);
    SB(); a2[0][1] = mfma32(af[0][0], hq[0][1], a2[0][1]); SB();
    af[1][1] = lda(std::integral_constant<int, 29>{}, 1);
    SB(); a2[1][0] = mfma32(af[0][1], hq[0][0], a2[1][0]); SB();
    SB(); a2[1][1] = mfma32(af[0][1], hq[0][1], a2[1][1]);
    asm volatile("" ::"v"(af[0][0]), "v"(af[0][1])); SB();
    SB(); a2[0][0] = mfma32(af[1][0], hq[0][2], a2[0][0]); SB();
    af[0][0] = lda(std::integral_constant<int, 30>{}, 0);
    SB(); a2[0][1] = mfma32(af[1][0], hq[0][3], a2[0][1]); SB();
    af[0][1] = lda(std::integral_constant<int, 30>{}, 1);
    SB(); a2[1][0] = mfma32(af[1][1], hq[0][2], a2[1][0]); SB();
    SB(); a2[1][1] = mfma32(af[1][1], hq[0][3], a2[1][1]);
    asm volatile("" ::"v"(af[1][0]), "v"(af[1][1])); SB();
    asm volatile("s_waitcnt vmcnt(0)" : "+v"(fl)::"memory");
    if (lane == 0) __hip_atomic_store((GAS u64*)(ws + WS_F2 + ((wg * 4 + q) * 2 + 1) * FLS), __builtin_bit_cast(u64, tag), RLX_AGENT);
    piece(std::integral_constant<int, 18>{}, std::integral_constant<int, 0>{});
    piece(std::integral_constant<int, 18>{}, std::integral_constant<int, 1>{});
    _Pragma("clang loop unroll(disable)") for (u32 sp = 0; sp < SPIN_CAP; ++sp) {
      if (wall(fl.x == tag.x && fl.y == tag.y)) break;
      __builtin_amdgcn_s_sleep(2);
      ld8_sc1(fl, fo + 0, rws, 0);
      asm volatile("s_waitcnt vmcnt(0)" : "+v"(fl)::"memory");
    }
    hb = hbase + sib(1) * 32768 + 0;
    ld16_sc1<0>(hq[1][0], lane * 16, rws, hb);
    ld16_sc1<1024>(hq[1][1], lane * 16, rws, hb);
    ld16_sc1<2048>(hq[1][2], lane * 16, rws, hb);
    ld16_sc1<3072>(hq[1][3], lane * 16, rws, hb);
    hb = hbase + sib(2) * 32768 + 0;
    ld16_sc1<0>(hq[2][0], lane * 16, rws, hb);
    ld16_sc1<1024>(hq[2][1], lane * 16, rws, hb);
    ld16_sc1<2048>(hq[2][2], lane * 16, rws, hb);
    ld16_sc1<3072>(hq[2][3], lane * 16, rws, hb);
    hb = hbase + sib(3) * 32768 + 0;
    ld16_sc1<0>(hq[0][0], lane * 16, rws, hb);
    ld16_sc1<1024>(hq[0][1], lane * 16, rws, hb);
    ld16_sc1<2048>(hq[0][2], lane * 16, rws, hb);
    ld16_sc1<3072>(hq[0][3], lane * 16, rws, hb);
    waitv4<8>(hq[1]);
    SB(); a2[0][0] = mfma32(af[0][0], hq[1][0], a2[0][0]); SB();
    af[1][0] = lda(std::integral_constant<int, 31>{}, 0);
    SB(); a2[0][1] = mfma32(af[0][0], hq[1][1], a2[0][1]); SB();
    af[1][1] = lda(std::integral_constant<int, 31>{}, 1);
    SB(); a2[1][0] = mfma32(af[0][1], hq[1][0], a2[1][0]); SB();
    piece(std::integral_constant<int, 18>{}, std::integral_constant<int, 2>{});
    SB(); a2[1][1] = mfma32(af[0][1], hq[1][1], a2[1][1]);
    asm volatile("" ::"v"(af[0][0]), "v"(af[0][1])); SB();
    piece(std::integral_constant<int, 18>{}, std::integral_constant<int, 3>{});
    SB(); a2[0][0] = mfma32(af[1][0], hq[1][2], a2[0][0]); SB();
    af[0][0] = lda(std::integral_constant<int, 32>{}, 0);
    SB(); a2[0][1] = mfma32(af[1][0], hq[1][3], a2[0][1]); SB();
    af[0][1] = lda(std::integral_constant<int, 32>{}, 1);
    SB(); a2[1][0] = mfma32(af[1][1], hq[1][2], a2[1][0]); SB();
    piece(std::integral_constant<int, 19>{}, std::integral_constant<int, 0>{});
    SB(); a2[1][1] = mfma32(af[1][1], hq[1][3], a2[1][1]);
    asm volatile("" ::"v"(af[1][0]), "v"(af[1][1])); SB();
    piece(std::integral_constant<int, 19>{}, std::integral_constant<int, 1>{});
    waitv4<8>(hq[2]);
    SB(); a2[0][0] = mfma32(af[0][0], hq[2][0], a2[0][0]); SB();
    af[1][0] = lda(std::integral_constant<int, 33>{}, 0);
    SB(); a2[0][1] = mfma32(af[0][0], hq[2][1], a2[0][1]); SB();
    af[1][1] = lda(std::integral_constant<int, 33>{}, 1);
    SB(); a2[1][0] = mfma32(af[0][1], hq[2][0], a2[1][0]); SB();
    piece(std::integral_constant<int, 19>{}, std::integral_constant<int, 2>{});
    hb = hbase + sib(4) * 32768 + 0;
    ld16_sc1<0>(hq[1][0], lane * 16, rws, hb);
    ld16_sc1<1024>(hq[1][1], lane * 16, rws, hb);
    SB(); a2[1][1] = mfma32(af[0][1], hq[2][1], a2[1][1]);
    asm volatile("" ::"v"(af[0][0]), "v"(af[0][1])); SB();
    piece(std::integral_constant<int, 19>{}, std::integral_constant<int, 3>{});
    ld16_sc1<2048>(hq[1][2], lane * 16, rws, hb);
    ld16_sc1<3072>(hq[1][3], lane * 16, rws, hb);
    SB(); a2[0][0] = mfma32(af[1][0], hq[2][2], a2[0][0]); SB();
    af[0][0] = lda(std::integral_constant<int, 34>{}, 0);
    SB(); a2[0][1] = mfma32(af[1][0], hq[2][3], a2[0][1]); SB();
    af[0][1] = lda(std::integral_constant<int, 34>{}, 1);
    SB(); a2[1][0] = mfma32(af[1][1], hq[2][2], a2[1][0]); SB();
    piece(std::integral_constant<int, 20>{}, std::integral_constant<int, 0>{});
    SB(); a2[1][1] = mfma32(af[1][1], hq[2][3], a2[1][1]);
    asm volatile("" ::"v"(af[1][0]), "v"(af[1][1])); SB();
    piece(std::integral_constant<int, 20>{}, std::integral_constant<int, 1>{});
    waitv4<12>(hq[0]);
    SB(); a2[0][0] = mfma32(af[0][0], hq[0][0], a2[0][0]); SB();
    af[1][0] = lda(std::integral_constant<int, 35>{}, 0);
    SB(); a2[0][1] = mfma32(af[0][0], hq[0][1], a2[0][1]); SB();
    af[1][1] = lda(std::integral_constant<int, 35>{}, 1);
    SB(); a2[1][0] = mfma32(af[0][1], hq[0][0], a2[1][0]); SB();
    piece(std::integral_constant<int, 20>{}, std::integral_constant<int, 2>{});
    hb = hbase + sib(5) * 32768 + 0;
    ld16_sc1<0>(hq[2][0], lane * 16, rws, hb);
    ld16_sc1<1024>(hq[2][1], lane * 16, rws, hb);
    SB(); a2[1][1] = mfma32(af[0][1], hq[0][1], a2[1][1]);
    asm volatile("" ::"v"(af[0][0]), "v"(af[0][1])); SB();
    piece(std::integral_constant<int, 20>{}, std::integral_constant<int, 3>{});
    ld16_sc1<2048>(hq[2][2], lane * 16, rws, hb);
    ld16_sc1<3072>(hq[2][3], lane * 16, rws, hb);
    SB(); a2[0][0] = mfma32(af[1][0], hq[0][2], a2[0][0]); SB();
    waitv<16>();
    af[0][0] = lda(std::integral_constant<int, 36>{}, 0);
    SB(); a2[0][1] = mfma32(af[1][0], hq[0][3], a2[0][1]); SB();
    af[0][1] = lda(std::integral_constant<int, 36>{}, 1);
    SB(); a2[1][0] = mfma32(af[1][1], hq[0][2], a2[1][0]); SB();
    piece(std::integral_constant<int, 21>{}, std::integral_constant<int, 0>{});
    SB(); a2[1][1] = mfma32(af[1][1], hq[0][3], a2[1][1]);
    asm volatile("" ::"v"(af[1][0]), "v"(af[1][1])); SB();
    piece(std::integral_constant<int, 21>{}, std::integral_constant<int, 1>{});
    ld8_sc1(fl, fo + 128, rws, 0);
    waitv4<11>(hq[1]);
    SB(); a2[0][0] = mfma32(af[0][0], hq[1][0], a2[0][0]); SB();
    af[1][0] = lda(std::integral_constant<int, 37>{}, 0);
    SB(); a2[0][1] = mfma32(af[0][0], hq[1][1], a2[0][1]); SB();
    af[1][1] = lda(std::integral_constant<int, 37>{}, 1);
    SB(); a2[1][0] = mfma32(af[0][1], hq[1][0], a2[1][0]); SB();
    piece(std::integral_constant<int, 21>{}, std::integral_constant<int, 2>{});
    hb = hbase + sib(6) * 32768 + 0;
    ld16_sc1<0>(hq[0][0], lane * 16, rws, hb);
    ld16_sc1<1024>(hq[0][1], lane * 16, rws, hb);
    SB(); a2[1][1] = mfma32(af[0][1], hq[1][1], a2[1][1]);
    asm volatile("" ::"v"(af[0][0]), "v"(af[0][1])); SB();
    piece(std::integral_constant<int, 21>{}, std::integral_constant<int, 3>{});
    ld16_sc1<2048>(hq[0][2], lane * 16, rws, hb);
    ld16_sc1<3072>(hq[0][3], lane * 16, rws, hb);
    SB(); a2[0][0] = mfma32(af[1][0], hq[1][2], a2[0][0]); SB();
    waitv<19>();
    af[0][0] = lda(std::integral_constant<int, 38>{}, 0);
    SB(); a2[0][1] = mfma32(af[1][0], hq[1][3], a2[0][1]); SB();
    af[0][1] = lda(std::integral_constant<int, 38>{}, 1);
    SB(); a2[1][0] = mfma32(af[1][1], hq[1][2], a2[1][0]); SB();
    piece(std::integral_constant<int, 22>{}, std::integral_constant<int, 0>{});
    SB(); a2[1][1] = mfma32(af[1][1], hq[1][3], a2[1][1]);
    asm volatile("" ::"v"(af[1][0]), "v"(af[1][1])); SB();
    piece(std::integral_constant<int, 22>{}, std::integral_constant<int, 1>{});
    asm volatile("s_waitcnt vmcnt(8)" : "+v"(fl)::"memory");
    _Pragma("clang loop unroll(disable)") for (u32 sp = 0; sp < SPIN_CAP; ++sp) {
      if (wall(fl.x == tag.x && fl.y == tag.y)) break;
      __builtin_amdgcn_s_sleep(2);
      ld8_sc1(fl, fo + 128, rws, 0);
      asm volatile("s_waitcnt vmcnt(0)" : "+v"(fl)::"memory");
    }
    waitv4<11>(hq[2]);
    SB(); a2[0][0] = mfma32(af[0][0], hq[2][0], a2[0][0]); SB();
    af[1][0] = lda(std::integral_constant<int, 39>{}, 0);
    SB(); a2[0][1] = mfma32(af[0][0], hq[2][1], a2[0][1]); SB();
    af[1][1] = lda(std::integral_constant<int, 39>{}, 1);
    SB(); a2[1][0] = mfma32(af[0][1], hq[2][0], a2[1][0]); SB();
    piece(std::integral_constant<int, 22>{}, std::integral_constant<int, 2>{});
    hb = hbase + sib(1) * 32768 + 4096;
    ld16_sc1<0>(hq[1][0], lane * 16, rws, hb);
    ld16_sc1<1024>(hq[1][1], lane * 16, rws, hb);
    SB(); a2[1][1] = mfma32(af[0][1], hq[2][1], a2[1][1]);
    asm volatile("" ::"v"(af[0][0]), "v"(af[0][1])); SB();
    piece(std::integral_constant<int, 22>{}, std::integral_constant<int, 3>{});
    ld16_sc1<2048>(hq[1][2], lane * 16, rws, hb);
    ld16_sc1<3072>(hq[1][3], lane * 16, rws, hb);
    SB(); a2[0][0] = mfma32(af[1][0], hq[2][2], a2[0][0]); SB();
    waitv<19>();
    af[0][0] = lda(std::integral_constant<int, 40>{}, 0);
    SB(); a2[0][1] = mfma32(af[1][0], hq[2][3], a2[0][1]); SB();
    af[0][1] = lda(std::integral_constant<int, 40>{}, 1);
    SB(); a2[1][0] = mfma32(af[1][1], hq[2][2], a2[1][0]); SB();
    piece(std::integral_constant<int, 23>{}, std::integral_constant<int, 0>{});
    SB(); a2[1][1] = mfma32(af[1][1], hq[2][3], a2[1][1]);
    asm volatile("" ::"v"(af[1][0]), "v"(af[1][1])); SB();
    piece(std::integral_constant<int, 23>{}, std::integral_constant<int, 1>{});
    waitv4<10>(hq[0]);
    SB(); a2[0][0] = mfma32(af[0][0], hq[0][0], a2[0][0]); SB();
    af[1][0] = lda(std::integral_constant<int, 41>{}, 0);
    SB(); a2[0][1] = mfma32(af[0][0], hq[0][1], a2[0][1]); SB();
    af[1][1] = lda(std::integral_constant<int, 41>{}, 1);
    SB(); a2[1][0] = mfma32(af[0][1], hq[0][0], a2[1][0]); SB();
    piece(std::integral_constant<int, 23>{}, std::integral_constant<int, 2>{});
    hb = hbase + sib(2) * 32768 + 4096;
    ld16_sc1<0>(hq[2][0], lane * 16, rws, hb);
    ld16_sc1<1024>(hq[2][1], lane * 16, rws, hb);
    SB(); a2[1][1] = mfma32(af[0][1], hq[0][1], a2[1][1]);
    asm volatile("" ::"v"(af[0][0]), "v"(af[0][1])); SB();
    piece(std::integral_constant<int, 23>{}, std::integral_constant<int, 3>{});
    ld16_sc1<2048>(hq[2][2], lane * 16, rws, hb);
    ld16_sc1<3072>(hq[2][3], lane * 16, rws, hb);
    SB(); a2[0][0] = mfma32(af[1][0], hq[0][2], a2[0][0]); SB();
    waitv<18>();
    af[0][0] = lda(std::integral_constant<int, 42>{}, 0);
    SB(); a2[0][1] = mfma32(af[1][0], hq[0][3], a2[0][1]); SB();
    af[0][1] = lda(std::integral_constant<int, 42>{}, 1);
    SB(); a2[1][0] = mfma32(af[1][1], hq[0][2], a2[1][0]); SB();
    piece(std::integral_constant<int, 24>{}, std::integral_constant<int, 0>{});
    SB(); a2[1][1] = mfma32(af[1][1], hq[0][3], a2[1][1]);
    asm volatile("" ::"v"(af[1][0]), "v"(af[1][1])); SB();
    piece(std::integral_constant<int, 24>{}, std::integral_constant<int, 1>{});
    SB(); a2[0][0] = mfma32(af[0][0], hq[3][0], a2[0][0]); SB();
    af[1][0] = lda(std::integral_constant<int, 43>{}, 0);
    SB(); a2[0][1] = mfma32(af[0][0], hq[3][1], a2[0][1]); SB();
    af[1][1] = lda(std::integral_constant<int, 43>{}, 1);
    SB(); a2[1][0] = mfma32(af[0][1], hq[3][0], a2[1][0]); SB();
    piece(std::integral_constant<int, 24>{}, std::integral_constant<int, 2>{});
    hb = hbase + sib(3) * 32768 + 4096;
    ld16_sc1<0>(hq[0][0], lane * 16, rws, hb);
    ld16_sc1<1024>(hq[0][1], lane * 16, rws, hb);
    SB(); a2[1][1] = mfma32(af[0][1], hq[3][1], a2[1][1]);
    asm volatile("" ::"v"(af[0][0]), "v"(af[0][1])); SB();
    piece(std::integral_constant<int, 24>{}, std::integral_constant<int, 3>{});
    ld16_sc1<2048>(hq[0][2], lane * 16, rws, hb);
    ld16_sc1<3072>(hq[0][3], lane * 16, rws, hb);
    SB(); a2[0][0] = mfma32(af[1][0], hq[3][2], a2[0][0]); SB();
    waitv<18>();
    af[0][0] = lda(std::integral_constant<int, 44>{}, 0);
    SB(); a2[0][1] = mfma32(af[1][0], hq[3][3], a2[0][1]); SB();
    af[0][1] = lda(std::integral_constant<int, 44>{}, 1);
    SB(); a2[1][0] = mfma32(af[1][1], hq[3][2], a2[1][0]); SB();
    piece(std::integral_constant<int, 25>{}, std::integral_constant<int, 0>{});
    SB(); a2[1][1] = mfma32(af[1][1], hq[3][3], a2[1][1]);
    asm volatile("" ::"v"(af[1][0]), "v"(af[1][1])); SB();
    piece(std::integral_constant<int, 25>{}, std::integral_constant<int, 1>{});
    waitv4<18>(hq[1]);
    SB(); a2[0][0] = mfma32(af[0][0], hq[1][0], a2[0][0]); SB();
    af[1][0] = lda(std::integral_constant<int, 45>{}, 0);
    SB(); a2[0][1] = mfma32(af[0][0], hq[1][1], a2[0][1]); SB();
    af[1][1] = lda(std::integral_constant<int, 45>{}, 1);
    SB(); a2[1][0] = mfma32(af[0][1], hq[1][0], a2[1][0]); SB();
    piece(std::integral_constant<int, 25>{}, std::integral_constant<int, 2>{});
    hb = hbase + sib(4) * 32768 + 4096;
    ld16_sc1<0>(hq[3][0], lane * 16, rws, hb);
    ld16_sc1<1024>(hq[3][1], lane * 16, rws, hb);
    SB(); a2[1][1] = mfma32(af[0][1], hq[1][1], a2[1][1]);
    asm volatile("" ::"v"(af[0][0]), "v"(af[0][1])); SB();
    piece(std::integral_constant<int, 25>{}, std::integral_constant<int, 3>{});
    ld16_sc1<2048>(hq[3][2], lane * 16, rws, hb);
    ld16_sc1<3072>(hq[3][3], lane * 16, rws, hb);
    SB(); a2[0][0] = mfma32(af[1][0], hq[1][2], a2[0][0]); SB();
    waitv<18>();
    af[0][0] = lda(std::integral_constant<int, 46>{}, 0);
    SB(); a2[0][1] = mfma32(af[1][0], hq[1][3], a2[0][1]); SB();
    af[0][1] = lda(std::integral_constant<int, 46>{}, 1);
    SB(); a2[1][0] = mfma32(af[1][1], hq[1][2], a2[1][0]); SB();
    piece(std::integral_constant<int, 26>{}, std::integral_constant<int, 0>{});
    SB(); a2[1][1] = mfma32(af[1][1], hq[1][3], a2[1][1]);
    asm volatile("" ::"v"(af[1][0]), "v"(af[1][1])); SB();
    piece(std::integral_constant<int, 26>{}, std::integral_constant<int, 1>{});
    waitv4<18>(hq[2]);
    SB(); a2[0][0] = mfma32(af[0][0], hq[2][0], a2[0][0]); SB();
    af[1][0] = lda(std::integral_constant<int, 47>{}, 0);
    SB(); a2[0][1] = mfma32(af[0][0], hq[2][1], a2[0][1]); SB();
    af[1][1] = lda(std::integral_constant<int, 47>{}, 1);
    SB(); a2[1][0] = mfma32(af[0][1], hq[2][0], a2[1][0]); SB();
    piece(std::integral_constant<int, 26>{}, std::integral_constant<int, 2>{});
    hb = hbase + sib(5) * 32768 + 4096;
    ld16_sc1<0>(hq[1][0], lane * 16, rws, hb);
    ld16_sc1<1024>(hq[1][1], lane * 16, rws, hb);
    SB(); a2[1][1] = mfma32(af[0][1], hq[2][1], a2[1][1]);
    asm volatile("" ::"v"(af[0][0]), "v"(af[0][1])); SB();
    piece(std::integral_constant<int, 26>{}, std::integral_constant<int, 3>{});
    ld16_sc1<2048>(hq[1][2], lane * 16, rws, hb);
    ld16_sc1<3072>(hq[1][3], lane * 16, rws, hb);
    SB(); a2[0][0] = mfma32(af[1][0], hq[2][2], a2[0][0]); SB();
    waitv<18>();
    af[0][0] = lda(std::integral_constant<int, 48>{}, 0);
    SB(); a2[0][1] = mfma32(af[1][0], hq[2][3], a2[0][1]); SB();
    af[0][1] = lda(std::integral_constant<int, 48>{}, 1);
    SB(); a2[1][0] = mfma32(af[1][1], hq[2][2], a2[1][0]); SB();
    piece(std::integral_constant<int, 27>{}, std::integral_constant<int, 0>{});
    SB(); a2[1][1] = mfma32(af[1][1], hq[2][3], a2[1][1]);
    asm volatile("" ::"v"(af[1][0]), "v"(af[1][1])); SB();
    piece(std::integral_constant<int, 27>{}, std::integral_constant<int, 1>{});
    waitv4<18>(hq[0]);
    SB(); a2[0][0] = mfma32(af[0][0], hq[0][0], a2[0][0]); SB();
    af[1][0] = lda(std::integral_constant<int, 49>{}, 0);
    SB(); a2[0][1] = mfma32(af[0][0], hq[0][1], a2[0][1]); SB();
    af[1][1] = lda(std::integral_constant<int, 49>{}, 1);
    SB(); a2[1][0] = mfma32(af[0][1], hq[0][0], a2[1][0]); SB();
    piece(std::integral_constant<int, 27>{}, std::integral_constant<int, 2>{});
    hb = hbase + sib(6) * 32768 + 4096;
    ld16_sc1<0>(hq[2][0], lane * 16, rws, hb);
    ld16_sc1<1024>(hq[2][1], lane * 16, rws, hb);
    SB(); a2[1][1] = mfma32(af[0][1], hq[0][1], a2[1][1]);
    asm volatile("" ::"v"(af[0][0]), "v"(af[0][1])); SB();
    piece(std::integral_constant<int, 27>{}, std::integral_constant<int, 3>{});
    ld16_sc1<2048>(hq[2][2], lane * 16, rws, hb);
    ld16_sc1<3072>(hq[2][3], lane * 16, rws, hb);
    if (lane == 0) __hip_atomic_fetch_add(lp<u32>(L_SYNC + 4 * S_F1), 1u, RLX_WG);
    SB(); a2[0][0] = mfma32(af[1][0], hq[0][2], a2[0][0]); SB();
    waitv<18>();
    af[0][0] = lda(std::integral_constant<int, 50>{}, 0);
    SB(); a2[0][1] = mfma32(af[1][0], hq[0][3], a2[0][1]); SB();
    af[0][1] = lda(std::integral_constant<int, 50>{}, 1);
    SB(); a2[1][0] = mfma32(af[1][1], hq[0][2], a2[1][0]); SB();
    SB(); a2[1][1] = mfma32(af[1][1], hq[0][3], a2[1][1]);
    asm volatile("" ::"v"(af[1][0]), "v"(af[1][1])); SB();
    waitv4<16>(hq[3]);
    SB(); a2[0][0] = mfma32(af[0][0], hq[3][0], a2[0][0]); SB();
    af[1][0] = lda(std::integral_constant<int, 51>{}, 0);
    SB(); a2[0][1] = mfma32(af[0][0], hq[3][1], a2[0][1]); SB();
    af[1][1] = lda(std::integral_constant<int, 51>{}, 1);
    SB(); a2[1][0] = mfma32(af[0][1], hq[3][0], a2[1][0]); SB();
    SB(); a2[1][1] = mfma32(af[0][1], hq[3][1], a2[1][1]);
    asm volatile("" ::"v"(af[0][0]), "v"(af[0][1])); SB();
    SB(); a2[0][0] = mfma32(af[1][0], hq[3][2], a2[0][0]); SB();
    waitv<10>();
    af[0][0] = lda(std::integral_constant<int, 52>{}, 0);
    SB(); a2[0][1] = mfma32(af[1][0], hq[3][3], a2[0][1]); SB();
    af[0][1] = lda(std::integral_constant<int, 52>{}, 1);
    SB(); a2[1][0] = mfma32(af[1][1], hq[3][2], a2[1][0]); SB();
    SB(); a2[1][1] = mfma32(af[1][1], hq[3][3], a2[1][1]);
    asm volatile("" ::"v"(af[1][0]), "v"(af[1][1])); SB();
    waitv4<8>(hq[1]);
    SB(); a2[0][0] = mfma32(af[0][0], hq[1][0], a2[0][0]); SB();
    af[1][0] = lda(std::integral_constant<int, 53>{}, 0);
    SB(); a2[0][1] = mfma32(af[0][0], hq[1][1], a2[0][1]); SB();
    af[1][1] = lda(std::integral_constant<int, 53>{}, 1);
    SB(); a2[1][0] = mfma32(af[0][1], hq[1][0], a2[1][0]); SB();
    SB(); a2[1][1] = mfma32(af[0][1], hq[1][1], a2[1][1]);
    asm volatile("" ::"v"(af[0][0]), "v"(af[0][1])); SB();
    SB(); a2[0][0] = mfma32(af[1][0], hq[1][2], a2[0][0]); SB();
    waitv<2>();
    af[0][0] = lda(std::integral_constant<int, 54>{}, 0);
    SB(); a2[0][1] = mfma32(af[1][0], hq[1][3], a2[0][1]); SB();
    af[0][1] = lda(std::integral_constant<int, 54>{}, 1);
    SB(); a2[1][0] = mfma32(af[1][1], hq[1][2], a2[1][0]); SB();
    SB(); a2[1][1] = mfma32(af[1][1], hq[1][3], a2[1][1]);
    asm volatile("" ::"v"(af[1][0]), "v"(af[1][1])); SB();
    waitv4<0>(hq[2]);
    SB(); a2[0][0] = mfma32(af[0][0], hq[2][0], a2[0][0]); SB();
    af[1][0] = lda(std::integral_constant<int, 55>{}, 0);
    SB(); a2[0][1] = mfma32(af[0][0], hq[2][1], a2[0][1]); SB();
    af[1][1] = lda(std::integral_constant<int, 55>{}, 1);
    SB(); a2[1][0] = mfma32(af[0][1], hq[2][0], a2[1][0]); SB();
    SB(); a2[1][1] = mfma32(af[0][1], hq[2][1], a2[1][1]);
    asm volatile("" ::"v"(af[0][0]), "v"(af[0][1])); SB();
    SB(); a2[0][0] = mfma32(af[1][0], hq[2][2], a2[0][0]); SB();
    SB(); a2[0][1] = mfma32(af[1][0], hq[2][3], a2[0][1]); SB();
    SB(); a2[1][0] = mfma32(af[1][1], hq[2][2], a2[1][0]); SB();
    SB(); a2[1][1] = mfma32(af[1][1], hq[2][3], a2[1][1]);
    asm volatile("" ::"v"(af[1][0]), "v"(af[1][1])); SB();
    u32 lz = lane;
    asm volatile("" : "+v"(lz));
#pragma unroll
    for (int c = 0; c < 2; ++c)
#pragma unroll
      for (int t = 0; t < 2; ++t)
#pragma unroll
        for (int k = 0; k < 4; ++k)
          *lp<f32x4>(ring + (32 * t + (lz & 31)) * 256 + (((8 * c + 2 * k + (lz >> 5)) ^ (lz & 7)) << 4)) =
              f32x4{a2[c][t][4 * k], a2[c][t][4 * k + 1], a2[c][t][4 * k + 2], a2[c][t][4 * k + 3]};
  } else {
    const int sv = wave - 4, cl = sv >> 2, sq = sv & 3, ch = 2 * wg + cl;
    __builtin_amdgcn_s_setprio(2);
    const u32 kg = L_KG + sv * 4096;
    const u32 po = ch * SN + sq * 64 + lane;
    const u16 pAr = Are[po], pAi = Aim[po], pBr = Bre[po], pBi = Bim[po], pCr = Cre[po], pCi = Cim[po];
    const float ldt = bf1(logdt[ch]);
    u32 vz = 0;
    asm("" : "+v"(vz));
    const v4u dk = *(const v4u*)(Dsk + (u32)(tj * 64 + sv * 8) + vz);
    const u16 bv = lane < 32 ? b1[(u32)(tj * 256 + sv * 32 + lane)] : b2[(u32)(tj * 64 + sv * 8 + (lane & 7))];
    asm volatile("" ::"v"((u32)bv), "v"((u32)pAr), "v"((u32)pAi), "v"((u32)pBr), "v"((u32)pBi), "v"((u32)pCr), "v"((u32)pCi), "v"(dk));
    const float dt = rbf(__builtin_amdgcn_exp2f(ldt * 1.4426950408889634f));
    const float bre = dt * bf1(pBr), bim = dt * bf1(pBi), cre = bf1(pCr), cim = bf1(pCi);
    const float ar2 = (dt * bf1(pAr)) * 1.4426950408889634f, rev = (dt * bf1(pAi)) * 0.15915494309189535f;
    cf cb{cre * bre - cim * bim, cre * bim + cim * bre};
    const bool fast = !wany(ar2 > 0.f);
    float us = 1.f, bound = 0.f;
    if (fast) {
      const float mx = wmax_nn(__builtin_fmaxf(__builtin_fabsf(cb.x), __builtin_fabsf(cb.y)));
      int eb = 267 - (int)(__builtin_bit_cast(u32, mx) >> 23);
      eb = eb > 253 ? 253 : eb;
      cb.x *= __builtin_bit_cast(float, (u32)eb << 23); cb.y *= __builtin_bit_cast(float, (u32)eb << 23);
      us = __builtin_bit_cast(float, (u32)(254 - eb) << 23);
      bound = mx * 91.f;
    }
    cf z1 = cexp_k(ar2, rev, 1.f), z8 = cexp_k(ar2, rev, 8.f), zc = cexp_k(ar2, rev, 128.f);
    float kscale_inv;
    {
      f32x4 ka[3] = {f32x4{0.f, 0.f, 0.f, 0.f}, f32x4{0.f, 0.f, 0.f, 0.f}, f32x4{0.f, 0.f, 0.f, 0.f}};
      if (fast) kgen_fast(ka, kg, lane, z1, z8, zc, cb); else kgen_slow(ka, lane, ar2, rev, cb);
      if (!fast) {
        float mx = __builtin_fabsf(ka[2][0]);
#pragma unroll
        for (int tl = 0; tl < 3; ++tl) { mx = __builtin_fmaxf(mx, __builtin_fmaxf(__builtin_fmaxf(__builtin_fabsf(ka[tl][0]), __builtin_fabsf(ka[tl][1])), __builtin_fmaxf(__builtin_fabsf(ka[tl][2]), __builtin_fabsf(ka[tl][3])))); ka[tl] *= 6.103515625e-05f; }
        bound = wmax_nn(mx); us = 268435456.f;
      }
#pragma unroll
      for (int tl = 0; tl < 3; ++tl) *lp<f32x4>(kg + tl * 1024 + lane * 16) = ka[tl];
      if (lane == 0) { *lp<float>(L_KM + sv * 4) = bound; *lp<float>(L_KM + 32 + sv * 4) = us; }
      lds_arrive(S_KP + cl, lane);
      __builtin_amdgcn_s_setprio(0);
    }
    lds_wait(S_LT, 4);
    const v2u tag = *lp<v2u>(L_TAG);
    if (sv < 7) {
      const u32 a = L_LT + ((g & 1) * 4 + (l15 >> 2)) * 960 + sv * 128 + (g >> 1) * 32 + (lane & 3) * 8;
      const v2u t0 = trd(a), t1 = trd(a + 64);
      lds_arrive(S_LR, lane);
      const u32 o = WS_XNT + (ti * 8 + tj) * 64 + sv * (32 * 16384) + xvs;
      stg(t0.x, t0.y, tag, rws, o); stg(t1.x, t1.y, tag, rws, o + 16 * 16384);
    }
    lds_wait(S_LR, 7);
    if (lane < 32) *lp<float>(L_B1 + (sv * 32 + lane) * 4) = bf1(bv); else if (lane < 40) *lp<float>(L_B2 + (sv * 8 + (lane & 7)) * 4) = bf1(bv);
    lds_arrive(S_AB, lane);
    const int m = sq * 64 + lane;
    v4u xr;
    {
      const int it = sq * 48 + lane, tl = it >> 6, c = it & 15, gg = (it >> 4) & 3;
      const bool md = tl == 1 && (gg & 1);
      const int base = tl == 0 ? 4 * (gg & 1) + 128 * (gg >> 1) + 8 * c : tl == 2 ? 512 + 128 * (gg & 1) + 64 * (gg >> 1) + c : md ? 384 + (gg >> 1) + 2 * c : 256 + 32 * gg + 4 * c;
      u32 o = L_KT + cl * KTS + base * 2, s1 = tl == 2 ? 32 : md ? 64 : 2, pa = L_KG + cl * 16384 + it * 16;
      asm volatile("" : "+v"(o), "+v"(s1), "+v"(pa));
      lds_wait(S_KP + cl, 4);
      const f32x4 m4 = *lp<f32x4>(L_KM + cl * 16), u4 = *lp<f32x4>(L_KM + 32 + cl * 16);
      int eb = 268 - (int)(__builtin_bit_cast(u32, (m4[0] + m4[1]) + (m4[2] + m4[3])) >> 23);
      eb = eb > 253 ? 253 : (eb < 15 ? 15 : eb);
      const f32x4 sc = u4 * __builtin_bit_cast(float, (u32)(eb - 14) << 23);
      kscale_inv = __builtin_bit_cast(float, (u32)(254 - eb) << 23);
      if (lane < 48) {
        const f32x4 v = *lp<f32x4>(pa) * sc[0] + *lp<f32x4>(pa + 4096) * sc[1] + *lp<f32x4>(pa + 8192) * sc[2] + *lp<f32x4>(pa + 12288) * sc[3];
        *lp<_Float16>(o) = (_Float16)v[0]; *lp<_Float16>(o + s1) = (_Float16)v[1]; *lp<_Float16>(o + 2 * s1) = (_Float16)v[2]; *lp<_Float16>(o + 3 * s1) = (_Float16)v[3];
      }
      lds_wait(S_XRDY, 4);
      xr = *lp<v4u>(L_XT + lane * 896 + tj * 128 + ((sv ^ ((lane >> 1) & 7)) << 4));
      lds_arrive(S_KQ + cl, lane);
    }
    v4u kr;
    {
      u32 qa[8];
#pragma unroll
      for (int e = 0; e < 4; ++e) {
        const int l0 = 2047 - (m * 8 + 2 * e), l1 = l0 - 1;
        const u32 pb = pk_bf((float)l0, (float)l1);
        qa[2 * e] = L_KT + cl * KTS + 2 * (l0 < 128 ? l0 : (int)(pb & 0xffffu) - 17024);
        qa[2 * e + 1] = L_KT + cl * KTS + 2 * (l1 < 128 ? l1 : (int)(pb >> 16) - 17024);
      }
      asm volatile("" : "+v"(qa[0]), "+v"(qa[1]), "+v"(qa[2]), "+v"(qa[3]), "+v"(qa[4]), "+v"(qa[5]), "+v"(qa[6]), "+v"(qa[7]));
      lds_wait(S_KQ + cl, 4);
#pragma unroll
      for (int e = 0; e < 4; ++e) kr[e] = (u32)*lp<u16>(qa[2 * e]) | ((u32)*lp<u16>(qa[2 * e + 1]) << 16);
    }
    {
      const int mu = sv * 32 + (lane >> 1);
      const u32 uo = WS_XNT + wg * 16384 + sv * 2048 + lane * 32, uo2 = lane >= 62 && sv < 7 ? uo + 64 : uo;
      v4u u0, u1, u2;
      lds_wait<LSL>(S_G1, 4);
#pragma clang loop unroll(disable)
      for (u32 sp = 0; sp < SPIN_CAP; ++sp) {
        u0 = ldg(rws, uo); u1 = ldg(rws, uo + 16); u2 = u0;
        if (lane >= 62) u2 = ldg(rws, uo2);
        if (wall(gok(u0, tag) && gok(u1, tag) && gok(u2, tag))) break;
        asm volatile("s_sleep %0" ::"i"(SLP_U) : "memory");
      }
      const v4u um = {u0.x, u0.y, u1.x, u1.y};
      v2u uh = {(u32)__builtin_amdgcn_ds_bpermute((lane + 2) * 4, (int)um.x), (u32)__builtin_amdgcn_ds_bpermute((lane + 2) * 4, (int)um.y)};
      if (lane >= 62) uh = sv < 7 ? v2u{u2.x, u2.y} : v2u{0u, 0u};
      lds_wait(S_KQ + (cl ^ 1), 4); lds_wait(S_G1, 4);
      *lp<v4u>(L_KR + cl * KRS + m * 16) = kr;
      if (sq == 3 && lane < 30) *lp<v4u>(L_KR + cl * KRS + 4096 + lane * 16) = v4u{0u, 0u, 0u, 0u};
      auto putc = [&](int mm, v4u a, v2u h) {
        const u32 o = L_UC + (lane & 1) * 4 * UCS + (mm + 2) * 16;
        const u32 s0 = __builtin_amdgcn_alignbit(a.y, a.x, 16), s1 = __builtin_amdgcn_alignbit(a.z, a.y, 16), s2 = __builtin_amdgcn_alignbit(a.w, a.z, 16),
                  s3 = __builtin_amdgcn_alignbit(h.x, a.w, 16), s4 = __builtin_amdgcn_alignbit(h.y, h.x, 16);
        *lp<v4u>(o) = v4u{s0, s1, s2, s3};
        u32 ay = a.y, az = a.z, aw = a.w, hx = h.x;
        asm("" : "+v"(ay), "+v"(az), "+v"(aw), "+v"(hx));
        *lp<v4u>(o + UCS) = v4u{ay, az, aw, hx};
        *lp<v4u>(o + 2 * UCS) = v4u{s1, s2, s3, s4};
        *lp<v4u>(o + 3 * UCS) = v4u{a.z, a.w, h.x, h.y};
      };
      putc(mu, um, uh);
      if (mu == 0) { putc(-1, v4u{0u, 0u, 0u, 0u}, v2u{um.x, um.y}); putc(-2, v4u{0u, 0u, 0u, 0u}, v2u{0u, 0u}); }
    }
    lds_arrive(S_CV, lane);
    v4u mrv = ldg(rws, WS_ST + (ti * 64 + lane) * 32 + 16);
    lds_wait(S_CV, 8);
    {
      const u32 ua = L_UC + (cl * 4 + (l15 >> 2)) * UCS + g * 16 + (lane & 3) * 8;
      const u32 kaddr = L_KR + cl * KRS + (2032 - 16 * l15 + 8 * g) * 2;
      f32x4 y0 = {0.f, 0.f, 0.f, 0.f}, y1 = y0;
      if (sq < 2) {
        conv_seg(y0, y1, ua, kaddr, sq ? 4 : 6, sq ? 0 : 2, 0);
        *lp<f32x4>(kg + 2048 + lane * 16) = y0; *lp<f32x4>(kg + 3072 + lane * 16) = y1;
        lds_arrive(S_HP + cl * 2 + sq, lane);
        y0 = f32x4{0.f, 0.f, 0.f, 0.f}; y1 = y0;
      }
      conv_seg(y0, y1, ua, kaddr, 2 * sq, 2 * sq + 1, sq < 2 ? 0 : (sq == 2 ? 1 : 3));
      if (sq >= 2) {
        lds_wait(S_HP + cl * 2 + 3 - sq, 1);
        const u32 hp = L_KG + (cl * 4 + 3 - sq) * 4096 + 2048 + lane * 16;
        y0 += *lp<f32x4>(hp); y1 += *lp<f32x4>(hp + 1024);
      }
      y0 *= kscale_inv; y1 *= kscale_inv;
      const u32 yo = WS_YC + ch * 8192 + sq * 2048 + (4 * l15 + g) * 16;
      stg(pk_bf(y0[0], y0[1]), pk_bf(y0[2], y0[3]), tag, rws, yo);
      stg(pk_bf(y1[0], y1[1]), pk_bf(y1[2], y1[3]), tag, rws, yo + 1024);
    }
    lds_arrive(S_CD, lane);
    {
      const u32 yo = WS_YC + (tj * 64 + sv * 8 + (lane >> 4)) * 8192 + ti * 256 + l15 * 16;
#pragma clang loop unroll(disable)
      for (u32 sp = 0; sp < SPIN_CAP; ++sp) {
        if (wall(gok(mrv, tag))) break;
        asm volatile("s_sleep 1" ::: "memory");
        mrv = ldg(rws, WS_ST + (ti * 64 + lane) * 32 + 16);
      }
      const f32x4 mr = __builtin_bit_cast(f32x4, mrv);
      f32x4 r0, r1v;
      {
        const f32x4 c0 = *lp<f32x4>(L_B2 + sv * 32), c1 = *lp<f32x4>(L_B2 + sv * 32 + 16);
#pragma unroll
        for (int k = 0; k < 8; ++k) {
          const float xv = (k & 1) ? bf_hi(xr[k >> 1]) : bf_lo(xr[k >> 1]), dv = (k & 1) ? bf_hi(dk[k >> 1]) : bf_lo(dk[k >> 1]);
          const float v = __builtin_fmaf((xv - mr.x) * mr.y, dv, xv + (k < 4 ? c0[k & 3] : c1[k & 3]));
          if (k < 4) r0[k] = v; else r1v[k - 4] = v;
        }
      }
      asm volatile("" : "+v"(r0), "+v"(r1v));
      lds_wait<LSL>(S_F1, 4);
      v4u y0, y1;
#pragma clang loop unroll(disable)
      for (u32 sp = 0; sp < SPIN_CAP; ++sp) {
        y0 = ldg(rws, yo); y1 = ldg(rws, yo + 32768);
        if (wall(gok(y0, tag) && gok(y1, tag))) break;
        asm volatile("s_sleep 2" ::: "memory");
      }
      lds_wait(S_CD, 8);
      const u32 yt = L_YT + sv * 8 * 144;
      *lp<v2u>(yt + g * 144 + l15 * 8) = v2u{y0.x, y0.y}; *lp<v2u>(yt + (g + 4) * 144 + l15 * 8) = v2u{y1.x, y1.y};
      const u32 a = yt + (l15 >> 2) * 144 + g * 32 + (lane & 3) * 8;
      const v4u yv = trd2(a, a + 4 * 144);
      r0 += f32x4{bf_lo(yv[0]), bf_hi(yv[0]), bf_lo(yv[1]), bf_hi(yv[1])};
      r1v += f32x4{bf_lo(yv[2]), bf_hi(yv[2]), bf_lo(yv[3]), bf_hi(yv[3])};
      *lp<f32x4>(L_UC + lane * 272 + sv * 32) = r0; *lp<f32x4>(L_UC + lane * 272 + sv * 32 + 16) = r1v;
    }
  }
  WAITL();
  __builtin_amdgcn_s_barrier();
  asm volatile("" ::: "memory");
  waitv<0>();
  if (tid < 512) {
    u32 tz = tid;
    asm volatile("" : "+v"(tz));
    const u32 t = tz >> 3, dq = tz & 7;
    f32x4 s0 = *lp<f32x4>(L_UC + t * 272 + dq * 32), s1 = *lp<f32x4>(L_UC + t * 272 + dq * 32 + 16);
#pragma unroll
    for (int w = 0; w < 4; ++w) {
      s0 += *lp<f32x4>(L_RING + w * 16384 + t * 256 + (((2 * dq) ^ (t & 7)) << 4));
      s1 += *lp<f32x4>(L_RING + w * 16384 + t * 256 + (((2 * dq + 1) ^ (t & 7)) << 4));
    }
    const v4u o = {pk_bf(s0[0], s0[1]), pk_bf(s0[2], s0[3]), pk_bf(s1[0], s1[1]), pk_bf(s1[2], s1[3])};
    *(v4u*)(out + (ti * 64 + t) * SD + tj * 64 + dq * 8) = o;
  }
}

#if defined(__HIP_DEVICE_COMPILE__)
#pragma clang attribute pop
#endif

extern "C" void kernel_launch(void* const* d_in, const int* in_sizes, int n_in, void* d_out, int out_size, void* d_ws, size_t ws_size,
                              hipStream_t stream) {
  fused_kernel<<<NWG + NSV, NTHR, 0, stream>>>((const u16*)d_in[0], (const u16*)d_in[1], (const u16*)d_in[2], (const u16*)d_in[3], (const u16*)d_in[4],
                                         (const u16*)d_in[5], (const u16*)d_in[6], (const u16*)d_in[7], (const u16*)d_in[8], (const u16*)d_in[10],
                                         (const u16*)d_in[11], (const u16*)d_in[12], (const u16*)d_in[13], (u16*)d_out, (char*)d_ws);
}
```
